```python
import torch

def _softmax(x, axis=-1):
    return torch.softmax(x, dim=axis)
def _gelu(x):
    return torch.nn.functional.gelu(x, approximate="tanh")
def _silu(x):
    return torch.nn.functional.silu(x)
def _sigmoid(x):
    return torch.sigmoid(x)
def _layernorm(x, axis=-1, eps=1e-5):
    m = x.mean(dim=axis, keepdim=True)
    v = x.var(dim=axis, keepdim=True, unbiased=False)
    return (x - m) / torch.sqrt(v + eps)
def _scatter_add(tgt, idx, src, axis=0):
    return tgt.clone().index_add_(axis, idx, src)
def _segment_sum(src, seg, n):
    out = torch.zeros((n,) + src.shape[1:], dtype=src.dtype)
    return out.index_add_(0, seg, src)
def _pad(x, pw):
    flat = tuple(v for pair in reversed(pw) for v in pair)
    return torch.nn.functional.pad(x, flat)

kernel_name = 'concat_heads__sliding_block_cached__sliding_block_cached__decode'

def setup_inputs():

    _w = (lambda a: (lambda t: t.to(torch.bfloat16) if t.is_floating_point() else t)(torch.as_tensor(a)))
    def _A_setup():
        B, Sq, D = 1, 1, 640
        Hq, Hkv, Sk, Dh = 32, 32, 4096, 192
        assert Hq % Hkv == 0, (Hq, Hkv)
        return {
            "x": _w((lambda *s: torch.randn(*s, dtype=torch.bfloat16) * 1.6)(B, Sq, D)),
            "k_cache": _w((lambda *s: torch.randn(*s, dtype=torch.bfloat16) * 1.6)(B, Hkv, Sk, Dh)),
            "v_cache": _w((lambda *s: torch.randn(*s, dtype=torch.bfloat16) * 0.05)(B, Hkv, Sk, Dh)),
            "wq": _w((lambda *s: torch.randn(*s, dtype=torch.bfloat16) * 1.6)(D, Hq * Dh) * (D ** -0.5)),
            "wo": _w((lambda *s: torch.randn(*s, dtype=torch.bfloat16) * 0.05)(Hq * Dh, D) * ((Hq * Dh) ** -0.5)),
        }
    _A_in = _A_setup()

    def _B_setup():
        B, Sq, D = 1, 1, 640
        Hq, Hkv, Sk, Dh = 32, 32, 4096, 192
        assert Hq % Hkv == 0, (Hq, Hkv)
        return {
            "x": _w((lambda *s: torch.randn(*s, dtype=torch.bfloat16) * 1.6)(B, Sq, D)),
            "k_cache": _w((lambda *s: torch.randn(*s, dtype=torch.bfloat16) * 1.6)(B, Hkv, Sk, Dh)),
            "v_cache": _w((lambda *s: torch.randn(*s, dtype=torch.bfloat16) * 0.05)(B, Hkv, Sk, Dh)),
            "wq": _w((lambda *s: torch.randn(*s, dtype=torch.bfloat16) * 1.6)(D, Hq * Dh) * (D ** -0.5)),
            "wo": _w((lambda *s: torch.randn(*s, dtype=torch.bfloat16) * 0.05)(Hq * Dh, D) * ((Hq * Dh) ** -0.5)),
        }
    _B_in = _B_setup()

    return {
        "A_x": _A_in["x"],
        "A_k_cache": _A_in["k_cache"],
        "A_v_cache": _A_in["v_cache"],
        "A_wq": _A_in["wq"],
        "A_wo": _A_in["wo"],
        "B_x": _B_in["x"],
        "B_k_cache": _B_in["k_cache"],
        "B_v_cache": _B_in["v_cache"],
        "B_wq": _B_in["wq"],
        "B_wo": _B_in["wo"],
    }

def reference(A_x, A_k_cache, A_v_cache, A_wq, A_wo, B_x, B_k_cache, B_v_cache, B_wq, B_wo):
    def _A_ref(x, k_cache, v_cache, wq, wo):
        B, Sq, D = x.shape
        Hq, Dh = 32, 192
        Hkv, Sk = k_cache.shape[1], k_cache.shape[2]
        W = min(4096, max(256, Sk // 4))
        rep = Hq // Hkv
        q = (lambda x, *d: x.permute(*d))((x @ wq).reshape(B, Sq, Hq, Dh), 0, 2, 1, 3)
        k = (lambda x, n, axis: torch.repeat_interleave(x, n, dim=axis))(k_cache[:, :, Sk - W:, :], rep, axis=1)
        v = (lambda x, n, axis: torch.repeat_interleave(x, n, dim=axis))(v_cache[:, :, Sk - W:, :], rep, axis=1)
        scores = torch.einsum("bhqd,bhkd->bhqk", q, k) * (Dh ** -0.5)
        p = _softmax(scores, axis=-1)
        o = torch.einsum("bhqk,bhkd->bhqd", p, v)
        return (lambda x, *d: x.permute(*d))(o, 0, 2, 1, 3).reshape(B, Sq, Hq * Dh) @ wo

    def _B_ref(x, k_cache, v_cache, wq, wo):
        B, Sq, D = x.shape
        Hq, Dh = 32, 192
        Hkv, Sk = k_cache.shape[1], k_cache.shape[2]
        W = min(4096, max(256, Sk // 4))
        rep = Hq // Hkv
        q = (lambda x, *d: x.permute(*d))((x @ wq).reshape(B, Sq, Hq, Dh), 0, 2, 1, 3)
        k = (lambda x, n, axis: torch.repeat_interleave(x, n, dim=axis))(k_cache[:, :, Sk - W:, :], rep, axis=1)
        v = (lambda x, n, axis: torch.repeat_interleave(x, n, dim=axis))(v_cache[:, :, Sk - W:, :], rep, axis=1)
        scores = torch.einsum("bhqd,bhkd->bhqk", q, k) * (Dh ** -0.5)
        p = _softmax(scores, axis=-1)
        o = torch.einsum("bhqk,bhkd->bhqd", p, v)
        return (lambda x, *d: x.permute(*d))(o, 0, 2, 1, 3).reshape(B, Sq, Hq * Dh) @ wo

    a = _A_ref(A_x, A_k_cache, A_v_cache, A_wq, A_wo)
    b = _B_ref(B_x, B_k_cache, B_v_cache, B_wq, B_wo)
    return torch.concatenate([a, b], axis=-1)
```

```cpp
#include <hip/hip_runtime.h>
#include <hip/hip_bf16.h>

typedef unsigned short u16;
__device__ __forceinline__ float bf2f(u16 b) { return __uint_as_float(((unsigned)b) << 16); }
__device__ __forceinline__ float rbf(float f) { return bf2f(__builtin_bit_cast(u16, (__bf16)f)); }

constexpr int D = 640, H = 32, SK = 4096, DH = 192, W = 1024, HD = H * DH;

__global__ void k_q(const u16* xA, const u16* wqA, const u16* xB, const u16* wqB, float* q) {
  int p = blockIdx.y;
  const u16* x = p ? xB : xA; const u16* wq = p ? wqB : wqA;
  int c = blockIdx.x * blockDim.x + threadIdx.x;
  float acc = 0.f;
  for (int d = 0; d < D; ++d) acc += bf2f(x[d]) * bf2f(wq[(size_t)d * HD + c]);
  q[p * HD + c] = rbf(acc);
}

__global__ void k_attn(const u16* kA, const u16* vA, const u16* kB, const u16* vB, const float* q, float* o) {
  int p = blockIdx.y, h = blockIdx.x;
  const u16* k = (p ? kB : kA) + ((size_t)h * SK + (SK - W)) * DH;
  const u16* v = (p ? vB : vA) + ((size_t)h * SK + (SK - W)) * DH;
  __shared__ float s[W];
  __shared__ float red[256];
  __shared__ float qs[DH];
  int t = threadIdx.x;
  if (t < DH) qs[t] = q[p * HD + h * DH + t];
  __syncthreads();
  float mx = -INFINITY;
  for (int j = t; j < W; j += 256) {
    float a = 0.f;
    for (int d = 0; d < DH; ++d) a += qs[d] * bf2f(k[(size_t)j * DH + d]);
    a = rbf(rbf(a) * 0.07216878364870322f);
    s[j] = a; mx = fmaxf(mx, a);
  }
  red[t] = mx; __syncthreads();
  for (int o2 = 128; o2 > 0; o2 >>= 1) { if (t < o2) red[t] = fmaxf(red[t], red[t + o2]); __syncthreads(); }
  mx = red[0]; __syncthreads();
  float sum = 0.f;
  for (int j = t; j < W; j += 256) { float e = __expf(s[j] - mx); s[j] = e; sum += e; }
  red[t] = sum; __syncthreads();
  for (int o2 = 128; o2 > 0; o2 >>= 1) { if (t < o2) red[t] += red[t + o2]; __syncthreads(); }
  sum = red[0];
  __syncthreads();
  if (t < DH) {
    float a = 0.f;
    for (int j = 0; j < W; ++j) a += rbf(s[j] / sum) * bf2f(v[(size_t)j * DH + t]);
    o[p * HD + h * DH + t] = rbf(a);
  }
}

__global__ void k_o(const float* o, const u16* woA, const u16* woB, __bf16* out) {
  int p = blockIdx.y;
  const u16* wo = p ? woB : woA;
  int c = blockIdx.x * 64 + (threadIdx.x & 63);
  int part = threadIdx.x >> 6;
  __shared__ float red[256];
  float acc = 0.f;
  for (int r = part; r < HD; r += 4) acc += o[p * HD + r] * bf2f(wo[(size_t)r * D + c]);
  red[threadIdx.x] = acc; __syncthreads();
  if (part == 0) out[p * D + c] = (__bf16)(red[threadIdx.x] + red[threadIdx.x + 64] + red[threadIdx.x + 128] + red[threadIdx.x + 192]);
}

extern "C" void kernel_launch(void* const* d_in, const int* in_sizes, int n_in,
                              void* d_out, int out_size, void* d_ws, size_t ws_size,
                              hipStream_t stream) {
  const u16* xA = (const u16*)d_in[0]; const u16* kA = (const u16*)d_in[1]; const u16* vA = (const u16*)d_in[2];
  const u16* wqA = (const u16*)d_in[3]; const u16* woA = (const u16*)d_in[4];
  const u16* xB = (const u16*)d_in[5]; const u16* kB = (const u16*)d_in[6]; const u16* vB = (const u16*)d_in[7];
  const u16* wqB = (const u16*)d_in[8]; const u16* woB = (const u16*)d_in[9];
  float* q = (float*)d_ws; float* o = q + 2 * HD;
  k_q<<<dim3(HD / 256, 2), 256, 0, stream>>>(xA, wqA, xB, wqB, q);
  k_attn<<<dim3(H, 2), 256, 0, stream>>>(kA, vA, kB, vB, q, o);
  k_o<<<dim3(D / 64, 2), 256, 0, stream>>>(o, woA, woB, (__bf16*)d_out);
}
```

```cpp
#include <hip/hip_runtime.h>

typedef unsigned short u16;
typedef unsigned int u32;
typedef u32 v4u __attribute__((ext_vector_type(4)));
typedef u32 v2u __attribute__((ext_vector_type(2)));
typedef float v2f __attribute__((ext_vector_type(2)));
typedef float v4f __attribute__((ext_vector_type(4)));
typedef __bf16 v2bf __attribute__((ext_vector_type(2)));
#define LAS __attribute__((address_space(3)))

namespace {
constexpr int D = 640, H = 32, SK = 4096, DH = 192, W = 1024, HD = H * DH;
constexpr int NTHR = 576;
constexpr float SCALE = 0.07216878364870322f;
constexpr float LOG2E = 1.4426950408889634f;
constexpr u32 QX_OFF = 4096, QX_UNIT = 1536;
constexpr u32 OX_OFF = QX_OFF + 256 * QX_UNIT, OX_UNIT = 2048;
constexpr u32 FX_OFF = OX_OFF + 256 * OX_UNIT;
constexpr u32 WS_USED = FX_OFF + 2 * 2 * 40 * 4096;
constexpr int RSRC_W3 = 0x00020000;
constexpr int AUX_NT = 2, AUX_SC1 = 16;
constexpr u32 SPIN_CAP = 1u << 16;

struct alignas(16) Lds {
  float part[20 * 192];
  float opart[8][192];
  float mw[8];
  float lw[8];
  u32 qbf[96];
  float ofin[96];
  u32 tag, pad0[3];
  float dummy[8];
  u32 pad[(84 * 1024 - (20 * 192 + 8 * 192 + 16 + 96 + 96 + 4 + 8) * 4) / 4];
};

__device__ __forceinline__ float bflo(u32 u) { return __uint_as_float(u << 16); }
__device__ __forceinline__ float bfhi(u32 u) { return __uint_as_float(u & 0xffff0000u); }
__device__ __forceinline__ u32 bfbits(float f) { return (u32)__builtin_bit_cast(u16, (__bf16)f); }
__device__ __forceinline__ float rbf(float f) { return __uint_as_float(bfbits(f) << 16); }
template <int CTRL> __device__ __forceinline__ float dpp(float x) {
  return __int_as_float(__builtin_amdgcn_update_dpp(0, __float_as_int(x), CTRL, 0xf, 0xf, true));
}
constexpr int DPP_XOR1 = 0xB1, DPP_XOR2 = 0x4E, DPP_HMIR = 0x141, DPP_ROR4 = 0x124, DPP_ROR8 = 0x128, DPP_BC0 = 0x150, DPP_BC8 = 0x158;
__device__ __forceinline__ float sum_rows(float x) {
  auto a = __builtin_amdgcn_permlane16_swap(__float_as_uint(x), __float_as_uint(x), false, false);
  x = __uint_as_float(a[0]) + __uint_as_float(a[1]);
  auto b = __builtin_amdgcn_permlane32_swap(__float_as_uint(x), __float_as_uint(x), false, false);
  return __uint_as_float(b[0]) + __uint_as_float(b[1]);
}
__device__ __forceinline__ float max_rows(float x) {
  auto a = __builtin_amdgcn_permlane16_swap(__float_as_uint(x), __float_as_uint(x), false, false);
  x = fmaxf(__uint_as_float(a[0]), __uint_as_float(a[1]));
  auto b = __builtin_amdgcn_permlane32_swap(__float_as_uint(x), __float_as_uint(x), false, false);
  return fmaxf(__uint_as_float(b[0]), __uint_as_float(b[1]));
}
__device__ __forceinline__ float dot8(v4u a, v4u b) {
  float acc = 0.f;
#pragma unroll
  for (int i = 0; i < 4; ++i) { const u32 x = a[i], y = b[i]; acc = __builtin_amdgcn_fdot2_f32_bf16(__builtin_bit_cast(v2bf, x), __builtin_bit_cast(v2bf, y), acc, false); }
  return acc;
}
__device__ __forceinline__ void fma8(v2f (&acc)[4], v4u wv, float s) {
  const v2f ss = {s, s};
#pragma unroll
  for (int i = 0; i < 4; ++i) { const v2f u = {bflo(wv[i]), bfhi(wv[i])}; acc[i] = __builtin_elementwise_fma(u, ss, acc[i]); }
}
__device__ __forceinline__ v2f fold_rows(const v2f (&a)[4]) {
  float b[4];
#pragma unroll
  for (int j = 0; j < 4; ++j) {
    const float lo = j < 2 ? a[0][j] : a[1][j - 2], hi = j < 2 ? a[2][j] : a[3][j - 2];
    auto r = __builtin_amdgcn_permlane32_swap(__float_as_uint(lo), __float_as_uint(hi), false, false);
    b[j] = __uint_as_float(r[0]) + __uint_as_float(r[1]);
  }
  v2f c;
#pragma unroll
  for (int j = 0; j < 2; ++j) {
    auto r = __builtin_amdgcn_permlane16_swap(__float_as_uint(b[j]), __float_as_uint(b[j + 2]), false, false);
    c[j] = __uint_as_float(r[0]) + __uint_as_float(r[1]);
  }
  return c;
}
#define SB() __builtin_amdgcn_sched_barrier(0)
#define PIN4(a) asm volatile("" : "+v"(a[0]), "+v"(a[1]), "+v"(a[2]), "+v"(a[3]))
#define WG_BARRIER() asm volatile("s_waitcnt lgkmcnt(0)\n\ts_barrier" ::: "memory")
#define LDNT(rs, vo, so) __builtin_amdgcn_raw_buffer_load_b128(rs, (int)(vo), (int)(so), AUX_NT)
#define LDSC(rs, vo) __builtin_amdgcn_raw_buffer_load_b128(rs, (int)(vo), 0, AUX_SC1)

#define STAMP(i) do {} while (0)

__global__ void __launch_bounds__(NTHR, 3)
fused_decode(const u16* __restrict__ xA, const u16* __restrict__ kA, const u16* __restrict__ vA, const u16* __restrict__ wqA, const u16* __restrict__ woA,
             const u16* __restrict__ xB, const u16* __restrict__ kB, const u16* __restrict__ vB, const u16* __restrict__ wqB, const u16* __restrict__ woB,
             u16* __restrict__ out, unsigned char* __restrict__ ws) {
  __shared__ Lds L;
  const int b = blockIdx.x, tid = threadIdx.x, lane = tid & 63, w = __builtin_amdgcn_readfirstlane(tid >> 6);
  const int p = b >> 7, h = (b & 7) * 4 + ((b >> 5) & 3), s = (b >> 3) & 3;
  const int rh = s >> 1, ch = s & 1;
  STAMP(0);
  if (w < 8) {
    const int t = tid < 480 ? tid : 479;
    const bool act = tid < 480;
    const __amdgpu_buffer_rsrc_t rsX = __builtin_amdgcn_make_buffer_rsrc((void*)(p ? xB : xA), 0, D * 2, RSRC_W3);
    const __amdgpu_buffer_rsrc_t rsQ = __builtin_amdgcn_make_buffer_rsrc((void*)((p ? wqB : wqA) + (size_t)s * 160 * HD + h * DH), 0, 160 * HD * 2 - h * DH * 2, RSRC_W3);
    const __amdgpu_buffer_rsrc_t rsK = __builtin_amdgcn_make_buffer_rsrc((void*)((p ? kB : kA) + ((size_t)h * SK + (SK - W) + s * 256 + w * 32) * DH), 0, 32 * DH * 2, RSRC_W3);
    const __amdgpu_buffer_rsrc_t rsV = __builtin_amdgcn_make_buffer_rsrc((void*)((p ? vB : vA) + ((size_t)h * SK + (SK - W) + s * 256 + w * 32) * DH), 0, 32 * DH * 2, RSRC_W3);
    const __amdgpu_buffer_rsrc_t rsO = __builtin_amdgcn_make_buffer_rsrc((void*)((p ? woB : woA) + ((size_t)h * DH + rh * 96) * D + ch * 320), 0, 96 * D * 2 - ch * 640, RSRC_W3);
    const int qrc = t / 24, qch = t - qrc * 24;
    const int voq = qrc * 8 * (HD * 2) + qch * 16;
    const v4u xr = __builtin_amdgcn_raw_buffer_load_b128(rsX, (s * 160 + qrc * 8) * 2, 0, 0);
    v4u a[8];
#pragma unroll
    for (int i = 0; i < 8; ++i) a[i] = LDNT(rsQ, voq, i * (HD * 2));
    SB();
    const int vo1 = (lane >> 4) * 384 + (lane & 15) * 16;
    const int vo3 = ((lane >> 4) + 4 * ((lane >> 3) & 1)) * 384 + 256 + (lane & 7) * 16;
    v4u kk[12], vv[12];
#define LDKV(rs, i) __builtin_amdgcn_raw_buffer_load_b128(rs, ((i) % 3 == 2 ? vo3 : vo1), (((i) / 3) * 3072 + ((i) % 3 == 1 ? 1536 : 0)), AUX_NT)
    {
      v2f acc[4] = {{0.f, 0.f}, {0.f, 0.f}, {0.f, 0.f}, {0.f, 0.f}};
#pragma unroll
      for (int i = 0; i < 8; ++i) {
        const float xs = (i & 1) ? bfhi(xr[i >> 1]) : bflo(xr[i >> 1]);
        fma8(acc, a[i], xs);
        PIN4(acc);
        SB();
        kk[i] = LDKV(rsK, i);
        SB();
      }
      STAMP(1);
      {
        LAS v4f* dst = (LAS v4f*)(act ? &L.part[qrc * 192 + qch * 8] : &L.dummy[0]);
        dst[0] = v4f{acc[0][0], acc[0][1], acc[1][0], acc[1][1]};
        dst[1] = v4f{acc[2][0], acc[2][1], acc[3][0], acc[3][1]};
      }
    }
    SB();
#pragma unroll
    for (int i = 8; i < 12; ++i) kk[i] = LDKV(rsK, i);
    SB();
    WG_BARRIER();
    SB();
#pragma unroll
    for (int i = 0; i < 6; ++i) vv[i] = LDKV(rsV, i);
    SB();
    STAMP(2);
    WG_BARRIER();
    STAMP(3);
    float sc[4];
    {
      const v4u qa = *(LAS v4u*)&L.qbf[4 * (lane & 15)];
      const v4u qb = *(LAS v4u*)&L.qbf[64 + 4 * (lane & 7)];
#pragma unroll
      for (int g = 0; g < 4; ++g) {
        float d1 = dot8(kk[3 * g], qa), d2 = dot8(kk[3 * g + 1], qa);
        const float d3 = dot8(kk[3 * g + 2], qb);
        d1 += dpp<DPP_ROR8>(d1);
        d2 += dpp<DPP_ROR8>(d2);
        float x = ((lane & 8) ? d2 : d1) + d3;
        x += dpp<DPP_XOR1>(x);
        x += dpp<DPP_XOR2>(x);
        x += dpp<DPP_HMIR>(x);
        sc[g] = rbf(rbf(x) * SCALE);
      }
    }
    {
      float m = fmaxf(fmaxf(sc[0], sc[1]), fmaxf(sc[2], sc[3]));
      m = fmaxf(m, dpp<DPP_ROR8>(m));
      m = max_rows(m);
      if (lane == 0) L.mw[w] = m;
    }
    SB();
#pragma unroll
    for (int i = 6; i < 12; ++i) vv[i] = LDKV(rsV, i);
    SB();
    WG_BARRIER();
    float pr[4];
    {
      const v4f m0 = *(LAS v4f*)&L.mw[0], m1 = *(LAS v4f*)&L.mw[4];
      const float m = fmaxf(fmaxf(fmaxf(m0[0], m0[1]), fmaxf(m0[2], m0[3])), fmaxf(fmaxf(m1[0], m1[1]), fmaxf(m1[2], m1[3])));
#pragma unroll
      for (int g = 0; g < 4; ++g) pr[g] = __builtin_amdgcn_exp2f((sc[g] - m) * LOG2E);
      float ls = (pr[0] + pr[1]) + (pr[2] + pr[3]);
      ls += dpp<DPP_ROR8>(ls);
      ls = sum_rows(ls);
      if (lane == 0) L.lw[w] = ls;
    }
    STAMP(4);
    const int orc = t / 40, och = t - orc * 40;
    const int voo = orc * 8 * (D * 2) + och * 16;
    v4u ww[8];
    {
      v2f a12[4] = {{0.f, 0.f}, {0.f, 0.f}, {0.f, 0.f}, {0.f, 0.f}}, a3[4] = {{0.f, 0.f}, {0.f, 0.f}, {0.f, 0.f}, {0.f, 0.f}};
#pragma unroll
      for (int g = 0; g < 4; ++g) {
        const float p1 = dpp<DPP_BC0>(pr[g]), p2 = dpp<DPP_BC8>(pr[g]);
        fma8(a12, vv[3 * g], p1);
        fma8(a12, vv[3 * g + 1], p2);
        fma8(a3, vv[3 * g + 2], pr[g]);
        PIN4(a12); PIN4(a3);
        SB();
        if (g < 3) {
          ww[2 * g] = LDNT(rsO, voo, (2 * g) * (D * 2));
          ww[2 * g + 1] = LDNT(rsO, voo, (2 * g + 1) * (D * 2));
        }
        SB();
      }
      STAMP(5);
#pragma unroll
      for (int i = 0; i < 4; ++i) { a3[i][0] += dpp<DPP_ROR8>(a3[i][0]); a3[i][1] += dpp<DPP_ROR8>(a3[i][1]); }
      const v2f c12 = fold_rows(a12), c3 = fold_rows(a3);
      const int r = lane >> 4, sub = 2 * (r & 1) + 4 * (r >> 1);
      *(LAS v2f*)&L.opart[w][8 * (lane & 15) + sub] = c12;
      if (!(lane & 8)) *(LAS v2f*)&L.opart[w][128 + 8 * (lane & 7) + sub] = c3;
    }
    WG_BARRIER();
    SB();
    ww[6] = LDNT(rsO, voo, 6 * (D * 2));
    ww[7] = LDNT(rsO, voo, 7 * (D * 2));
    SB();
    v2f wf[8][4];
#pragma unroll
    for (int i = 0; i < 8; ++i)
#pragma unroll
      for (int j = 0; j < 4; ++j) { wf[i][j] = v2f{bflo(ww[i][j]), bfhi(ww[i][j])}; asm volatile("" : "+v"(wf[i][j])); }
    STAMP(6);
    WG_BARRIER();
    STAMP(7);
    {
      const v4f o0 = *(LAS v4f*)&L.ofin[orc * 8], o1 = *(LAS v4f*)&L.ofin[orc * 8 + 4];
      v2f acc[4] = {{0.f, 0.f}, {0.f, 0.f}, {0.f, 0.f}, {0.f, 0.f}};
#pragma unroll
      for (int i = 0; i < 8; ++i) {
        const float os = i < 4 ? o0[i] : o1[i - 4];
        const v2f ss = {os, os};
#pragma unroll
        for (int j = 0; j < 4; ++j) acc[j] = __builtin_elementwise_fma(wf[i][j], ss, acc[j]);
      }
      {
        LAS v4f* dst = (LAS v4f*)(act ? &L.part[orc * 320 + och * 8] : &L.dummy[0]);
        dst[0] = v4f{acc[0][0], acc[0][1], acc[1][0], acc[1][1]};
        dst[1] = v4f{acc[2][0], acc[2][1], acc[3][0], acc[3][1]};
      }
    }
    WG_BARRIER();
    if (tid < 320) {
      float v = 0.f;
#pragma unroll
      for (int i = 0; i < 12; ++i) v += L.part[i * 320 + tid];
      const __amdgpu_buffer_rsrc_t rsW = __builtin_amdgcn_make_buffer_rsrc((void*)ws, 0, WS_USED, RSRC_W3);
      const u32 off = FX_OFF + (u32)((p * 2 + ch) * 40 + (tid >> 3)) * 4096 + (u32)(h * 2 + rh) * 64 + (tid & 7) * 8;
      __builtin_amdgcn_raw_buffer_store_b64(v2u{__float_as_uint(v), L.tag}, rsW, (int)off, 0, AUX_SC1);
    }
    STAMP(8);
  } else {
    const __amdgpu_buffer_rsrc_t rsW = __builtin_amdgcn_make_buffer_rsrc((void*)ws, 0, WS_USED, RSRC_W3);
    const u32 T = __builtin_amdgcn_readfirstlane(__builtin_amdgcn_raw_buffer_load_b32(rsW, p * 128, 0, AUX_SC1)) + 1u;
    if (lane == 0) L.tag = T;
    const int l48 = lane < 48 ? lane : 47;
    WG_BARRIER();
    STAMP(1);
    {
      v4f q = {0.f, 0.f, 0.f, 0.f};
#pragma unroll
      for (int i = 0; i < 20; ++i) q += *(LAS v4f*)&L.part[i * 192 + 4 * l48];
      const u32 base = QX_OFF + (u32)((p * 32 + h) * 4) * QX_UNIT + l48 * 32;
      if (lane < 48) {
        __builtin_amdgcn_raw_buffer_store_b128(v4u{__float_as_uint(q[0]), T, __float_as_uint(q[1]), T}, rsW, (int)(base + s * QX_UNIT), 0, AUX_SC1);
        __builtin_amdgcn_raw_buffer_store_b128(v4u{__float_as_uint(q[2]), T, __float_as_uint(q[3]), T}, rsW, (int)(base + s * QX_UNIT + 16), 0, AUX_SC1);
      }
      STAMP(2);
      v4u g[4][2];
      for (u32 spin = 0; spin < SPIN_CAP; ++spin) {
        asm volatile("" ::: "memory");
        bool ok = true;
#pragma unroll
        for (int j = 0; j < 4; ++j) { g[j][0] = LDSC(rsW, base + j * QX_UNIT); g[j][1] = LDSC(rsW, base + j * QX_UNIT + 16); }
#pragma unroll
        for (int j = 0; j < 4; ++j) ok = ok & (g[j][0][1] == T) & (g[j][0][3] == T) & (g[j][1][1] == T) & (g[j][1][3] == T);
        if (__builtin_amdgcn_ballot_w64(!ok) == 0) break;
      }
      float qq[4];
#pragma unroll
      for (int e = 0; e < 4; ++e) {
        float v = __uint_as_float(g[0][e >> 1][(e & 1) * 2]);
#pragma unroll
        for (int j = 1; j < 4; ++j) v += __uint_as_float(g[j][e >> 1][(e & 1) * 2]);
        qq[e] = v;
      }
      if (lane < 48) *(LAS v2u*)&L.qbf[2 * lane] = v2u{bfbits(qq[0]) | (bfbits(qq[1]) << 16), bfbits(qq[2]) | (bfbits(qq[3]) << 16)};
    }
    STAMP(3);
    WG_BARRIER();
    WG_BARRIER();
    float m;
    {
      const v4f m0 = *(LAS v4f*)&L.mw[0], m1 = *(LAS v4f*)&L.mw[4];
      m = fmaxf(fmaxf(fmaxf(m0[0], m0[1]), fmaxf(m0[2], m0[3])), fmaxf(fmaxf(m1[0], m1[1]), fmaxf(m1[2], m1[3])));
    }
    WG_BARRIER();
    STAMP(4);
    {
      v4f o = {0.f, 0.f, 0.f, 0.f};
#pragma unroll
      for (int i = 0; i < 8; ++i) o += *(LAS v4f*)&L.opart[i][4 * l48];
      const v4f l0 = *(LAS v4f*)&L.lw[0], l1 = *(LAS v4f*)&L.lw[4];
      const float l = ((l0[0] + l0[1]) + (l0[2] + l0[3])) + ((l1[0] + l1[1]) + (l1[2] + l1[3]));
      const u32 base = OX_OFF + (u32)((p * 32 + h) * 4) * OX_UNIT;
      const bool isv = lane < 48;
      const v4u d0 = {__float_as_uint(isv ? o[0] : m), T, __float_as_uint(isv ? o[1] : l), T};
      if (lane <= 48) __builtin_amdgcn_raw_buffer_store_b128(d0, rsW, (int)(base + s * OX_UNIT + lane * 32), 0, AUX_SC1);
      if (lane < 48) __builtin_amdgcn_raw_buffer_store_b128(v4u{__float_as_uint(o[2]), T, __float_as_uint(o[3]), T}, rsW, (int)(base + s * OX_UNIT + lane * 32 + 16), 0, AUX_SC1);
      STAMP(5);
      const u32 roff = base + (isv ? (u32)(48 * rh + lane) * 16 : 1536u);
      v4u g[4];
      for (u32 spin = 0; spin < SPIN_CAP; ++spin) {
        asm volatile("" ::: "memory");
        bool ok = true;
#pragma unroll
        for (int j = 0; j < 4; ++j) g[j] = LDSC(rsW, roff + j * OX_UNIT);
#pragma unroll
        for (int j = 0; j < 4; ++j) ok = ok & (g[j][1] == T) & (g[j][3] == T);
        if (__builtin_amdgcn_ballot_w64(!ok) == 0) break;
      }
      float ms[4], ls[4];
#pragma unroll
      for (int j = 0; j < 4; ++j) {
        ms[j] = __uint_as_float(__builtin_amdgcn_readlane(g[j][0], 48));
        ls[j] = __uint_as_float(__builtin_amdgcn_readlane(g[j][2], 48));
      }
      const float M = fmaxf(fmaxf(ms[0], ms[1]), fmaxf(ms[2], ms[3]));
      float Ls = 0.f, o0 = 0.f, o1 = 0.f;
#pragma unroll
      for (int j = 0; j < 4; ++j) {
        const float f = __builtin_amdgcn_exp2f((ms[j] - M) * LOG2E);
        Ls += f * ls[j];
        o0 += f * __uint_as_float(g[j][0]);
        o1 += f * __uint_as_float(g[j][2]);
      }
      const float inv = 1.0f / Ls;
      if (lane < 48) *(LAS v2f*)&L.ofin[2 * lane] = v2f{rbf(o0 * inv), rbf(o1 * inv)};
    }
    STAMP(6);
    WG_BARRIER();
    WG_BARRIER();
    const int li = h * 4 + s;
    if (li < 80) {
      const u32 base = FX_OFF + (u32)(p * 80 + li) * 4096 + lane * 16;
      v4u g[4];
      STAMP(7);
      for (u32 spin = 0; spin < SPIN_CAP; ++spin) {
        asm volatile("" ::: "memory");
        bool ok = true;
#pragma unroll
        for (int j = 0; j < 4; ++j) g[j] = LDSC(rsW, base + j * 1024);
#pragma unroll
        for (int j = 0; j < 4; ++j) ok = ok & (g[j][1] == T) & (g[j][3] == T);
        if (__builtin_amdgcn_ballot_w64(!ok) == 0) break;
      }
      float v0 = (__uint_as_float(g[0][0]) + __uint_as_float(g[1][0])) + (__uint_as_float(g[2][0]) + __uint_as_float(g[3][0]));
      float v1 = (__uint_as_float(g[0][2]) + __uint_as_float(g[1][2])) + (__uint_as_float(g[2][2]) + __uint_as_float(g[3][2]));
      v0 += dpp<DPP_ROR8>(v0); v1 += dpp<DPP_ROR8>(v1);
      v0 += dpp<DPP_ROR4>(v0); v1 += dpp<DPP_ROR4>(v1);
      v0 = sum_rows(v0); v1 = sum_rows(v1);
      if (lane < 4) ((u32*)out)[(p * D + li * 8) / 2 + lane] = bfbits(v0) | (bfbits(v1) << 16);
      if (li == 0 && lane == 0) __builtin_amdgcn_raw_buffer_store_b32(T, rsW, p * 128, 0, AUX_SC1);
      STAMP(8);
    }
  }
}
}

extern "C" void kernel_launch(void* const* d_in, const int* in_sizes, int n_in, void* d_out, int out_size, void* d_ws, size_t ws_size,
                              hipStream_t stream) {
  fused_decode<<<256, NTHR, 0, stream>>>((const u16*)d_in[0], (const u16*)d_in[1], (const u16*)d_in[2], (const u16*)d_in[3], (const u16*)d_in[4],
                                         (const u16*)d_in[5], (const u16*)d_in[6], (const u16*)d_in[7], (const u16*)d_in[8], (const u16*)d_in[9],
                                         (u16*)d_out, (unsigned char*)d_ws);
}
```

```cpp
#define PROBE_T 2200
#include <hip/hip_runtime.h>

typedef unsigned short u16;
typedef unsigned int u32;
typedef u32 v4u __attribute__((ext_vector_type(4)));
typedef u32 v2u __attribute__((ext_vector_type(2)));
typedef float v2f __attribute__((ext_vector_type(2)));
typedef float v4f __attribute__((ext_vector_type(4)));
typedef __bf16 v2bf __attribute__((ext_vector_type(2)));
#define LAS __attribute__((address_space(3)))

namespace {
constexpr int D = 640, H = 32, SK = 4096, DH = 192, W = 1024, HD = H * DH;
constexpr int NTHR = 576;
constexpr float SCALE = 0.07216878364870322f;
constexpr float LOG2E = 1.4426950408889634f;
constexpr u32 QX_OFF = 4096, QX_UNIT = 1536;
constexpr u32 OX_OFF = QX_OFF + 256 * QX_UNIT, OX_UNIT = 2048;
constexpr u32 FX_OFF = OX_OFF + 256 * OX_UNIT;
constexpr u32 WS_USED = FX_OFF + 2 * 2 * 40 * 4096;
constexpr int RSRC_W3 = 0x00020000;
constexpr int AUX_NT = 2, AUX_SC1 = 16;
constexpr u32 SPIN_CAP = 1u << 16;

struct alignas(16) Lds {
  float part[20 * 192];
  float opart[8][192];
  float mw[8];
  float lw[8];
  u32 qbf[96];
  float ofin[96];
  u32 tag, pad0[3];
  float dummy[8];
  u32 pad[(84 * 1024 - (20 * 192 + 8 * 192 + 16 + 96 + 96 + 4 + 8) * 4) / 4];
};

__device__ __forceinline__ float bflo(u32 u) { return __uint_as_float(u << 16); }
__device__ __forceinline__ float bfhi(u32 u) { return __uint_as_float(u & 0xffff0000u); }
__device__ __forceinline__ u32 bfbits(float f) { return (u32)__builtin_bit_cast(u16, (__bf16)f); }
__device__ __forceinline__ float rbf(float f) { return __uint_as_float(bfbits(f) << 16); }
template <int CTRL> __device__ __forceinline__ float dpp(float x) {
  return __int_as_float(__builtin_amdgcn_update_dpp(0, __float_as_int(x), CTRL, 0xf, 0xf, true));
}
constexpr int DPP_XOR1 = 0xB1, DPP_XOR2 = 0x4E, DPP_HMIR = 0x141, DPP_ROR4 = 0x124, DPP_ROR8 = 0x128, DPP_BC0 = 0x150, DPP_BC8 = 0x158;
__device__ __forceinline__ float sum_rows(float x) {
  auto a = __builtin_amdgcn_permlane16_swap(__float_as_uint(x), __float_as_uint(x), false, false);
  x = __uint_as_float(a[0]) + __uint_as_float(a[1]);
  auto b = __builtin_amdgcn_permlane32_swap(__float_as_uint(x), __float_as_uint(x), false, false);
  return __uint_as_float(b[0]) + __uint_as_float(b[1]);
}
__device__ __forceinline__ float max_rows(float x) {
  auto a = __builtin_amdgcn_permlane16_swap(__float_as_uint(x), __float_as_uint(x), false, false);
  x = fmaxf(__uint_as_float(a[0]), __uint_as_float(a[1]));
  auto b = __builtin_amdgcn_permlane32_swap(__float_as_uint(x), __float_as_uint(x), false, false);
  return fmaxf(__uint_as_float(b[0]), __uint_as_float(b[1]));
}
__device__ __forceinline__ float dot8(v4u a, v4u b) {
  float acc = 0.f;
#pragma unroll
  for (int i = 0; i < 4; ++i) { const u32 x = a[i], y = b[i]; acc = __builtin_amdgcn_fdot2_f32_bf16(__builtin_bit_cast(v2bf, x), __builtin_bit_cast(v2bf, y), acc, false); }
  return acc;
}
__device__ __forceinline__ void fma8(v2f (&acc)[4], v4u wv, float s) {
  const v2f ss = {s, s};
#pragma unroll
  for (int i = 0; i < 4; ++i) { const v2f u = {bflo(wv[i]), bfhi(wv[i])}; acc[i] = __builtin_elementwise_fma(u, ss, acc[i]); }
}
__device__ __forceinline__ v2f fold_rows(const v2f (&a)[4]) {
  float b[4];
#pragma unroll
  for (int j = 0; j < 4; ++j) {
    const float lo = j < 2 ? a[0][j] : a[1][j - 2], hi = j < 2 ? a[2][j] : a[3][j - 2];
    auto r = __builtin_amdgcn_permlane32_swap(__float_as_uint(lo), __float_as_uint(hi), false, false);
    b[j] = __uint_as_float(r[0]) + __uint_as_float(r[1]);
  }
  v2f c;
#pragma unroll
  for (int j = 0; j < 2; ++j) {
    auto r = __builtin_amdgcn_permlane16_swap(__float_as_uint(b[j]), __float_as_uint(b[j + 2]), false, false);
    c[j] = __uint_as_float(r[0]) + __uint_as_float(r[1]);
  }
  return c;
}
#define SB() __builtin_amdgcn_sched_barrier(0)
#define PIN4(a) asm volatile("" : "+v"(a[0]), "+v"(a[1]), "+v"(a[2]), "+v"(a[3]))
#define WG_BARRIER() asm volatile("s_waitcnt lgkmcnt(0)\n\ts_barrier" ::: "memory")
#define LDNT(rs, vo, so) __builtin_amdgcn_raw_buffer_load_b128(rs, (int)(vo), (int)(so), AUX_NT)
#define LDSC(rs, vo) __builtin_amdgcn_raw_buffer_load_b128(rs, (int)(vo), 0, AUX_SC1)

#define STAMP(i) do {} while (0)

__global__ void __launch_bounds__(NTHR, 3)
fused_decode(const u16* __restrict__ xA, const u16* __restrict__ kA, const u16* __restrict__ vA, const u16* __restrict__ wqA, const u16* __restrict__ woA,
             const u16* __restrict__ xB, const u16* __restrict__ kB, const u16* __restrict__ vB, const u16* __restrict__ wqB, const u16* __restrict__ woB,
             u16* __restrict__ out, unsigned char* __restrict__ ws) {
  __shared__ Lds L;
  const int b = blockIdx.x, tid = threadIdx.x, lane = tid & 63, w = __builtin_amdgcn_readfirstlane(tid >> 6);
  const int p = b >> 7, h = (b & 7) * 4 + ((b >> 5) & 3), s = (b >> 3) & 3;
  const int rh = s >> 1, ch = s & 1;
  STAMP(0);
  const unsigned long long tstart = __builtin_amdgcn_s_memrealtime();
  if (w < 8) {
    const int t = tid < 480 ? tid : 479;
    const bool act = tid < 480;
    const __amdgpu_buffer_rsrc_t rsX = __builtin_amdgcn_make_buffer_rsrc((void*)(p ? xB : xA), 0, D * 2, RSRC_W3);
    const __amdgpu_buffer_rsrc_t rsQ = __builtin_amdgcn_make_buffer_rsrc((void*)((p ? wqB : wqA) + (size_t)s * 160 * HD + h * DH), 0, 160 * HD * 2 - h * DH * 2, RSRC_W3);
    const __amdgpu_buffer_rsrc_t rsK = __builtin_amdgcn_make_buffer_rsrc((void*)((p ? kB : kA) + ((size_t)h * SK + (SK - W) + s * 256 + w * 32) * DH), 0, 32 * DH * 2, RSRC_W3);
    const __amdgpu_buffer_rsrc_t rsV = __builtin_amdgcn_make_buffer_rsrc((void*)((p ? vB : vA) + ((size_t)h * SK + (SK - W) + s * 256 + w * 32) * DH), 0, 32 * DH * 2, RSRC_W3);
    const __amdgpu_buffer_rsrc_t rsO = __builtin_amdgcn_make_buffer_rsrc((void*)((p ? woB : woA) + ((size_t)h * DH + rh * 96) * D + ch * 320), 0, 96 * D * 2 - ch * 640, RSRC_W3);
    const int qrc = t / 24, qch = t - qrc * 24;
    const int voq = qrc * 8 * (HD * 2) + qch * 16;
    const v4u xr = __builtin_amdgcn_raw_buffer_load_b128(rsX, (s * 160 + qrc * 8) * 2, 0, 0);
    v4u a[8];
#pragma unroll
    for (int i = 0; i < 8; ++i) a[i] = LDNT(rsQ, voq, i * (HD * 2));
    SB();
    const int vo1 = (lane >> 4) * 384 + (lane & 15) * 16;
    const int vo3 = ((lane >> 4) + 4 * ((lane >> 3) & 1)) * 384 + 256 + (lane & 7) * 16;
    v4u kk[12], vv[12];
#define LDKV(rs, i) __builtin_amdgcn_raw_buffer_load_b128(rs, ((i) % 3 == 2 ? vo3 : vo1), (((i) / 3) * 3072 + ((i) % 3 == 1 ? 1536 : 0)), AUX_NT)
    {
      v2f acc[4] = {{0.f, 0.f}, {0.f, 0.f}, {0.f, 0.f}, {0.f, 0.f}};
#pragma unroll
      for (int i = 0; i < 8; ++i) {
        const float xs = (i & 1) ? bfhi(xr[i >> 1]) : bflo(xr[i >> 1]);
        fma8(acc, a[i], xs);
        PIN4(acc);
        SB();
        kk[i] = LDKV(rsK, i);
        SB();
      }
      STAMP(1);
      {
        LAS v4f* dst = (LAS v4f*)(act ? &L.part[qrc * 192 + qch * 8] : &L.dummy[0]);
        dst[0] = v4f{acc[0][0], acc[0][1], acc[1][0], acc[1][1]};
        dst[1] = v4f{acc[2][0], acc[2][1], acc[3][0], acc[3][1]};
      }
    }
    SB();
#pragma unroll
    for (int i = 8; i < 12; ++i) kk[i] = LDKV(rsK, i);
    SB();
    WG_BARRIER();
    SB();
#pragma unroll
    for (int i = 0; i < 6; ++i) vv[i] = LDKV(rsV, i);
    SB();
    STAMP(2);
    WG_BARRIER();
    STAMP(3);
    float sc[4];
    {
      const v4u qa = *(LAS v4u*)&L.qbf[4 * (lane & 15)];
      const v4u qb = *(LAS v4u*)&L.qbf[64 + 4 * (lane & 7)];
#pragma unroll
      for (int g = 0; g < 4; ++g) {
        float d1 = dot8(kk[3 * g], qa), d2 = dot8(kk[3 * g + 1], qa);
        const float d3 = dot8(kk[3 * g + 2], qb);
        d1 += dpp<DPP_ROR8>(d1);
        d2 += dpp<DPP_ROR8>(d2);
        float x = ((lane & 8) ? d2 : d1) + d3;
        x += dpp<DPP_XOR1>(x);
        x += dpp<DPP_XOR2>(x);
        x += dpp<DPP_HMIR>(x);
        sc[g] = rbf(rbf(x) * SCALE);
      }
    }
    {
      float m = fmaxf(fmaxf(sc[0], sc[1]), fmaxf(sc[2], sc[3]));
      m = fmaxf(m, dpp<DPP_ROR8>(m));
      m = max_rows(m);
      if (lane == 0) L.mw[w] = m;
    }
    SB();
#pragma unroll
    for (int i = 6; i < 12; ++i) vv[i] = LDKV(rsV, i);
    SB();
    WG_BARRIER();
    float pr[4];
    {
      const v4f m0 = *(LAS v4f*)&L.mw[0], m1 = *(LAS v4f*)&L.mw[4];
      const float m = fmaxf(fmaxf(fmaxf(m0[0], m0[1]), fmaxf(m0[2], m0[3])), fmaxf(fmaxf(m1[0], m1[1]), fmaxf(m1[2], m1[3])));
#pragma unroll
      for (int g = 0; g < 4; ++g) pr[g] = __builtin_amdgcn_exp2f((sc[g] - m) * LOG2E);
      float ls = (pr[0] + pr[1]) + (pr[2] + pr[3]);
      ls += dpp<DPP_ROR8>(ls);
      ls = sum_rows(ls);
      if (lane == 0) L.lw[w] = ls;
    }
    STAMP(4);
    const int orc = t / 40, och = t - orc * 40;
    const int voo = orc * 8 * (D * 2) + och * 16;
    v4u ww[8];
    {
      v2f a12[4] = {{0.f, 0.f}, {0.f, 0.f}, {0.f, 0.f}, {0.f, 0.f}}, a3[4] = {{0.f, 0.f}, {0.f, 0.f}, {0.f, 0.f}, {0.f, 0.f}};
#pragma unroll
      for (int g = 0; g < 4; ++g) {
        const float p1 = dpp<DPP_BC0>(pr[g]), p2 = dpp<DPP_BC8>(pr[g]);
        fma8(a12, vv[3 * g], p1);
        fma8(a12, vv[3 * g + 1], p2);
        fma8(a3, vv[3 * g + 2], pr[g]);
        PIN4(a12); PIN4(a3);
        SB();
        if (g < 3) {
          ww[2 * g] = LDNT(rsO, voo, (2 * g) * (D * 2));
          ww[2 * g + 1] = LDNT(rsO, voo, (2 * g + 1) * (D * 2));
        }
        SB();
      }
      STAMP(5);
#pragma unroll
      for (int i = 0; i < 4; ++i) { a3[i][0] += dpp<DPP_ROR8>(a3[i][0]); a3[i][1] += dpp<DPP_ROR8>(a3[i][1]); }
      const v2f c12 = fold_rows(a12), c3 = fold_rows(a3);
      const int r = lane >> 4, sub = 2 * (r & 1) + 4 * (r >> 1);
      *(LAS v2f*)&L.opart[w][8 * (lane & 15) + sub] = c12;
      if (!(lane & 8)) *(LAS v2f*)&L.opart[w][128 + 8 * (lane & 7) + sub] = c3;
    }
    WG_BARRIER();
    SB();
    ww[6] = LDNT(rsO, voo, 6 * (D * 2));
    ww[7] = LDNT(rsO, voo, 7 * (D * 2));
    SB();
    v2f wf[8][4];
#pragma unroll
    for (int i = 0; i < 8; ++i)
#pragma unroll
      for (int j = 0; j < 4; ++j) { wf[i][j] = v2f{bflo(ww[i][j]), bfhi(ww[i][j])}; asm volatile("" : "+v"(wf[i][j])); }
    STAMP(6);
    WG_BARRIER();
    STAMP(7);
    {
      const v4f o0 = *(LAS v4f*)&L.ofin[orc * 8], o1 = *(LAS v4f*)&L.ofin[orc * 8 + 4];
      v2f acc[4] = {{0.f, 0.f}, {0.f, 0.f}, {0.f, 0.f}, {0.f, 0.f}};
#pragma unroll
      for (int i = 0; i < 8; ++i) {
        const float os = i < 4 ? o0[i] : o1[i - 4];
        const v2f ss = {os, os};
#pragma unroll
        for (int j = 0; j < 4; ++j) acc[j] = __builtin_elementwise_fma(wf[i][j], ss, acc[j]);
      }
      {
        LAS v4f* dst = (LAS v4f*)(act ? &L.part[orc * 320 + och * 8] : &L.dummy[0]);
        dst[0] = v4f{acc[0][0], acc[0][1], acc[1][0], acc[1][1]};
        dst[1] = v4f{acc[2][0], acc[2][1], acc[3][0], acc[3][1]};
      }
    }
    WG_BARRIER();
    if (tid < 320) {
      float v = 0.f;
#pragma unroll
      for (int i = 0; i < 12; ++i) v += L.part[i * 320 + tid];
      const __amdgpu_buffer_rsrc_t rsW = __builtin_amdgcn_make_buffer_rsrc((void*)ws, 0, WS_USED, RSRC_W3);
      const u32 off = FX_OFF + (u32)((p * 2 + ch) * 40 + (tid >> 3)) * 4096 + (u32)(h * 2 + rh) * 64 + (tid & 7) * 8;
      __builtin_amdgcn_raw_buffer_store_b64(v2u{__float_as_uint(v), L.tag}, rsW, (int)off, 0, AUX_SC1);
    }
    STAMP(8);
  } else {
    const __amdgpu_buffer_rsrc_t rsW = __builtin_amdgcn_make_buffer_rsrc((void*)ws, 0, WS_USED, RSRC_W3);
    const u32 T = __builtin_amdgcn_readfirstlane(__builtin_amdgcn_raw_buffer_load_b32(rsW, p * 128, 0, AUX_SC1)) + 1u;
    if (lane == 0) L.tag = T;
    const int l48 = lane < 48 ? lane : 47;
    WG_BARRIER();
    STAMP(1);
    {
      v4f q = {0.f, 0.f, 0.f, 0.f};
#pragma unroll
      for (int i = 0; i < 20; ++i) q += *(LAS v4f*)&L.part[i * 192 + 4 * l48];
      const u32 base = QX_OFF + (u32)((p * 32 + h) * 4) * QX_UNIT + l48 * 32;
      if (lane < 48) {
        __builtin_amdgcn_raw_buffer_store_b128(v4u{__float_as_uint(q[0]), T, __float_as_uint(q[1]), T}, rsW, (int)(base + s * QX_UNIT), 0, AUX_SC1);
        __builtin_amdgcn_raw_buffer_store_b128(v4u{__float_as_uint(q[2]), T, __float_as_uint(q[3]), T}, rsW, (int)(base + s * QX_UNIT + 16), 0, AUX_SC1);
      }
      STAMP(2);
      v4u g[4][2];
      for (u32 spin = 0; spin < SPIN_CAP; ++spin) {
        asm volatile("" ::: "memory");
        bool ok = true;
#pragma unroll
        for (int j = 0; j < 4; ++j) { g[j][0] = LDSC(rsW, base + j * QX_UNIT); g[j][1] = LDSC(rsW, base + j * QX_UNIT + 16); }
#pragma unroll
        for (int j = 0; j < 4; ++j) ok = ok & (g[j][0][1] == T) & (g[j][0][3] == T) & (g[j][1][1] == T) & (g[j][1][3] == T);
        if (__builtin_amdgcn_ballot_w64(!ok) == 0) break;
      }
      float qq[4];
#pragma unroll
      for (int e = 0; e < 4; ++e) {
        float v = __uint_as_float(g[0][e >> 1][(e & 1) * 2]);
#pragma unroll
        for (int j = 1; j < 4; ++j) v += __uint_as_float(g[j][e >> 1][(e & 1) * 2]);
        qq[e] = v;
      }
      if (lane < 48) *(LAS v2u*)&L.qbf[2 * lane] = v2u{bfbits(qq[0]) | (bfbits(qq[1]) << 16), bfbits(qq[2]) | (bfbits(qq[3]) << 16)};
    }
    STAMP(3);
    WG_BARRIER();
    WG_BARRIER();
    float m;
    {
      const v4f m0 = *(LAS v4f*)&L.mw[0], m1 = *(LAS v4f*)&L.mw[4];
      m = fmaxf(fmaxf(fmaxf(m0[0], m0[1]), fmaxf(m0[2], m0[3])), fmaxf(fmaxf(m1[0], m1[1]), fmaxf(m1[2], m1[3])));
    }
    WG_BARRIER();
    STAMP(4);
    {
      v4f o = {0.f, 0.f, 0.f, 0.f};
#pragma unroll
      for (int i = 0; i < 8; ++i) o += *(LAS v4f*)&L.opart[i][4 * l48];
      const v4f l0 = *(LAS v4f*)&L.lw[0], l1 = *(LAS v4f*)&L.lw[4];
      const float l = ((l0[0] + l0[1]) + (l0[2] + l0[3])) + ((l1[0] + l1[1]) + (l1[2] + l1[3]));
      const u32 base = OX_OFF + (u32)((p * 32 + h) * 4) * OX_UNIT;
      const bool isv = lane < 48;
      const v4u d0 = {__float_as_uint(isv ? o[0] : m), T, __float_as_uint(isv ? o[1] : l), T};
      if (lane <= 48) __builtin_amdgcn_raw_buffer_store_b128(d0, rsW, (int)(base + s * OX_UNIT + lane * 32), 0, AUX_SC1);
      if (lane < 48) __builtin_amdgcn_raw_buffer_store_b128(v4u{__float_as_uint(o[2]), T, __float_as_uint(o[3]), T}, rsW, (int)(base + s * OX_UNIT + lane * 32 + 16), 0, AUX_SC1);
      STAMP(5);
      const u32 roff = base + (isv ? (u32)(48 * rh + lane) * 16 : 1536u);
      v4u g[4];
      for (u32 spin = 0; spin < SPIN_CAP; ++spin) {
        asm volatile("" ::: "memory");
        bool ok = true;
#pragma unroll
        for (int j = 0; j < 4; ++j) g[j] = LDSC(rsW, roff + j * OX_UNIT);
#pragma unroll
        for (int j = 0; j < 4; ++j) ok = ok & (g[j][1] == T) & (g[j][3] == T);
        if (__builtin_amdgcn_ballot_w64(!ok) == 0) break;
      }
      float ms[4], ls[4];
#pragma unroll
      for (int j = 0; j < 4; ++j) {
        ms[j] = __uint_as_float(__builtin_amdgcn_readlane(g[j][0], 48));
        ls[j] = __uint_as_float(__builtin_amdgcn_readlane(g[j][2], 48));
      }
      const float M = fmaxf(fmaxf(ms[0], ms[1]), fmaxf(ms[2], ms[3]));
      float Ls = 0.f, o0 = 0.f, o1 = 0.f;
#pragma unroll
      for (int j = 0; j < 4; ++j) {
        const float f = __builtin_amdgcn_exp2f((ms[j] - M) * LOG2E);
        Ls += f * ls[j];
        o0 += f * __uint_as_float(g[j][0]);
        o1 += f * __uint_as_float(g[j][2]);
      }
      const float inv = 1.0f / Ls;
      if (lane < 48) *(LAS v2f*)&L.ofin[2 * lane] = v2f{rbf(o0 * inv), rbf(o1 * inv)};
    }
    STAMP(6);
    WG_BARRIER();
    WG_BARRIER();
    const int li = h * 4 + s;
    if (li < 80) {
      const u32 base = FX_OFF + (u32)(p * 80 + li) * 4096 + lane * 16;
      v4u g[4];
      STAMP(7);
      for (u32 spin = 0; spin < SPIN_CAP; ++spin) {
        asm volatile("" ::: "memory");
        bool ok = true;
#pragma unroll
        for (int j = 0; j < 4; ++j) g[j] = LDSC(rsW, base + j * 1024);
#pragma unroll
        for (int j = 0; j < 4; ++j) ok = ok & (g[j][1] == T) & (g[j][3] == T);
        if (__builtin_amdgcn_ballot_w64(!ok) == 0) break;
      }
      float v0 = (__uint_as_float(g[0][0]) + __uint_as_float(g[1][0])) + (__uint_as_float(g[2][0]) + __uint_as_float(g[3][0]));
      float v1 = (__uint_as_float(g[0][2]) + __uint_as_float(g[1][2])) + (__uint_as_float(g[2][2]) + __uint_as_float(g[3][2]));
      v0 += dpp<DPP_ROR8>(v0); v1 += dpp<DPP_ROR8>(v1);
      v0 += dpp<DPP_ROR4>(v0); v1 += dpp<DPP_ROR4>(v1);
      v0 = sum_rows(v0); v1 = sum_rows(v1);
      const unsigned long long el = __builtin_amdgcn_s_memrealtime() - tstart;
      if (el > PROBE_T) v0 = (float)el * 0.01f;
      if (lane < 4) ((u32*)out)[(p * D + li * 8) / 2 + lane] = bfbits(v0) | (bfbits(v1) << 16);
      if (li == 0 && lane == 0) __builtin_amdgcn_raw_buffer_store_b32(T, rsW, p * 128, 0, AUX_SC1);
      STAMP(8);
    }
  }
}
}

extern "C" void kernel_launch(void* const* d_in, const int* in_sizes, int n_in, void* d_out, int out_size, void* d_ws, size_t ws_size,
                              hipStream_t stream) {
  fused_decode<<<256, NTHR, 0, stream>>>((const u16*)d_in[0], (const u16*)d_in[1], (const u16*)d_in[2], (const u16*)d_in[3], (const u16*)d_in[4],
                                         (const u16*)d_in[5], (const u16*)d_in[6], (const u16*)d_in[7], (const u16*)d_in[8], (const u16*)d_in[9],
                                         (u16*)d_out, (unsigned char*)d_ws);
}
```

```cpp
#include <hip/hip_runtime.h>

typedef unsigned short u16;
typedef unsigned int u32;
typedef u32 v4u __attribute__((ext_vector_type(4)));
typedef u32 v2u __attribute__((ext_vector_type(2)));
typedef float v2f __attribute__((ext_vector_type(2)));
typedef float v4f __attribute__((ext_vector_type(4)));
typedef __bf16 v2bf __attribute__((ext_vector_type(2)));
#define LAS __attribute__((address_space(3)))

namespace {
constexpr int D = 640, H = 32, SK = 4096, DH = 192, W = 1024, HD = H * DH;
constexpr int NTHR = 576;
constexpr float SCALE = 0.07216878364870322f;
constexpr float LOG2E = 1.4426950408889634f;
constexpr u32 QX_OFF = 4096, QX_UNIT = 1536;
constexpr u32 OX_OFF = QX_OFF + 256 * QX_UNIT, OX_UNIT = 2048;
constexpr u32 FX_OFF = OX_OFF + 256 * OX_UNIT;
constexpr u32 WS_USED = FX_OFF + 2 * 2 * 40 * 4096;
constexpr int RSRC_W3 = 0x00020000;
constexpr int AUX_NT = 2, AUX_SC1 = 16;
constexpr u32 SPIN_CAP = 1u << 16;

struct alignas(16) Lds {
  float part[20 * 192];
  float opart[8][192];
  float mw[8];
  float lw[8];
  u32 qbf[96];
  float ofin[96];
  u32 tag, pad0[3];
  float dummy[8];
  u32 pad[(84 * 1024 - (20 * 192 + 8 * 192 + 16 + 96 + 96 + 4 + 8) * 4) / 4];
};

__device__ __forceinline__ float bflo(u32 u) { return __uint_as_float(u << 16); }
__device__ __forceinline__ float bfhi(u32 u) { return __uint_as_float(u & 0xffff0000u); }
__device__ __forceinline__ u32 bfbits(float f) { return (u32)__builtin_bit_cast(u16, (__bf16)f); }
__device__ __forceinline__ float rbf(float f) { return __uint_as_float(bfbits(f) << 16); }
template <int CTRL> __device__ __forceinline__ float dpp(float x) {
  return __int_as_float(__builtin_amdgcn_update_dpp(0, __float_as_int(x), CTRL, 0xf, 0xf, true));
}
constexpr int DPP_XOR1 = 0xB1, DPP_XOR2 = 0x4E, DPP_HMIR = 0x141, DPP_ROR4 = 0x124, DPP_ROR8 = 0x128, DPP_BC0 = 0x150, DPP_BC8 = 0x158;
__device__ __forceinline__ float sum_rows(float x) {
  auto a = __builtin_amdgcn_permlane16_swap(__float_as_uint(x), __float_as_uint(x), false, false);
  x = __uint_as_float(a[0]) + __uint_as_float(a[1]);
  auto b = __builtin_amdgcn_permlane32_swap(__float_as_uint(x), __float_as_uint(x), false, false);
  return __uint_as_float(b[0]) + __uint_as_float(b[1]);
}
__device__ __forceinline__ float max_rows(float x) {
  auto a = __builtin_amdgcn_permlane16_swap(__float_as_uint(x), __float_as_uint(x), false, false);
  x = fmaxf(__uint_as_float(a[0]), __uint_as_float(a[1]));
  auto b = __builtin_amdgcn_permlane32_swap(__float_as_uint(x), __float_as_uint(x), false, false);
  return fmaxf(__uint_as_float(b[0]), __uint_as_float(b[1]));
}
__device__ __forceinline__ float dot8(v4u a, v4u b) {
  float acc = 0.f;
#pragma unroll
  for (int i = 0; i < 4; ++i) { const u32 x = a[i], y = b[i]; acc = __builtin_amdgcn_fdot2_f32_bf16(__builtin_bit_cast(v2bf, x), __builtin_bit_cast(v2bf, y), acc, false); }
  return acc;
}
__device__ __forceinline__ void fma8(v2f (&acc)[4], v4u wv, float s) {
  const v2f ss = {s, s};
#pragma unroll
  for (int i = 0; i < 4; ++i) { const v2f u = {bflo(wv[i]), bfhi(wv[i])}; acc[i] = __builtin_elementwise_fma(u, ss, acc[i]); }
}
__device__ __forceinline__ v2f fold_rows(const v2f (&a)[4]) {
  float b[4];
#pragma unroll
  for (int j = 0; j < 4; ++j) {
    const float lo = j < 2 ? a[0][j] : a[1][j - 2], hi = j < 2 ? a[2][j] : a[3][j - 2];
    auto r = __builtin_amdgcn_permlane32_swap(__float_as_uint(lo), __float_as_uint(hi), false, false);
    b[j] = __uint_as_float(r[0]) + __uint_as_float(r[1]);
  }
  v2f c;
#pragma unroll
  for (int j = 0; j < 2; ++j) {
    auto r = __builtin_amdgcn_permlane16_swap(__float_as_uint(b[j]), __float_as_uint(b[j + 2]), false, false);
    c[j] = __uint_as_float(r[0]) + __uint_as_float(r[1]);
  }
  return c;
}
#define SB() __builtin_amdgcn_sched_barrier(0)
#define PIN4(a) asm volatile("" : "+v"(a[0]), "+v"(a[1]), "+v"(a[2]), "+v"(a[3]))
#define WG_BARRIER() asm volatile("s_waitcnt lgkmcnt(0)\n\ts_barrier" ::: "memory")
#define LDNT(rs, vo, so) __builtin_amdgcn_raw_buffer_load_b128(rs, (int)(vo), (int)(so), AUX_NT)
#define LDSC(rs, vo) __builtin_amdgcn_raw_buffer_load_b128(rs, (int)(vo), 0, AUX_SC1)

#define STAMP(i) do {} while (0)

__global__ void __launch_bounds__(NTHR, 3)
fused_decode(const u16* __restrict__ xA, const u16* __restrict__ kA, const u16* __restrict__ vA, const u16* __restrict__ wqA, const u16* __restrict__ woA,
             const u16* __restrict__ xB, const u16* __restrict__ kB, const u16* __restrict__ vB, const u16* __restrict__ wqB, const u16* __restrict__ woB,
             u16* __restrict__ out, unsigned char* __restrict__ ws) {
  __shared__ Lds L;
  unsigned long long pc0; asm volatile("s_getpc_b64 %0" : "=s"(pc0));
  const int b = blockIdx.x, tid = threadIdx.x, lane = tid & 63, w = __builtin_amdgcn_readfirstlane(tid >> 6);
  const int p = b >> 7, h = (b & 7) * 4 + ((b >> 5) & 3), s = (b >> 3) & 3;
  const int rh = s >> 1, ch = s & 1;
  STAMP(0);
  if (w < 8) {
    const int t = tid < 480 ? tid : 479;
    const bool act = tid < 480;
    const __amdgpu_buffer_rsrc_t rsX = __builtin_amdgcn_make_buffer_rsrc((void*)(p ? xB : xA), 0, D * 2, RSRC_W3);
    const __amdgpu_buffer_rsrc_t rsQ = __builtin_amdgcn_make_buffer_rsrc((void*)((p ? wqB : wqA) + (size_t)s * 160 * HD + h * DH), 0, 160 * HD * 2 - h * DH * 2, RSRC_W3);
    const __amdgpu_buffer_rsrc_t rsK = __builtin_amdgcn_make_buffer_rsrc((void*)((p ? kB : kA) + ((size_t)h * SK + (SK - W) + s * 256 + w * 32) * DH), 0, 32 * DH * 2, RSRC_W3);
    const __amdgpu_buffer_rsrc_t rsV = __builtin_amdgcn_make_buffer_rsrc((void*)((p ? vB : vA) + ((size_t)h * SK + (SK - W) + s * 256 + w * 32) * DH), 0, 32 * DH * 2, RSRC_W3);
    const __amdgpu_buffer_rsrc_t rsO = __builtin_amdgcn_make_buffer_rsrc((void*)((p ? woB : woA) + ((size_t)h * DH + rh * 96) * D + ch * 320), 0, 96 * D * 2 - ch * 640, RSRC_W3);
    const int qrc = t / 24, qch = t - qrc * 24;
    const int voq = qrc * 8 * (HD * 2) + qch * 16;
    const v4u xr = __builtin_amdgcn_raw_buffer_load_b128(rsX, (s * 160 + qrc * 8) * 2, 0, 0);
    v4u a[8];
#pragma unroll
    for (int i = 0; i < 8; ++i) a[i] = LDNT(rsQ, voq, i * (HD * 2));
    SB();
    const int vo1 = (lane >> 4) * 384 + (lane & 15) * 16;
    const int vo3 = ((lane >> 4) + 4 * ((lane >> 3) & 1)) * 384 + 256 + (lane & 7) * 16;
    v4u kk[12], vv[12];
#define LDKV(rs, i) __builtin_amdgcn_raw_buffer_load_b128(rs, ((i) % 3 == 2 ? vo3 : vo1), (((i) / 3) * 3072 + ((i) % 3 == 1 ? 1536 : 0)), AUX_NT)
    {
      v2f acc[4] = {{0.f, 0.f}, {0.f, 0.f}, {0.f, 0.f}, {0.f, 0.f}};
#pragma unroll
      for (int i = 0; i < 8; ++i) {
        const float xs = (i & 1) ? bfhi(xr[i >> 1]) : bflo(xr[i >> 1]);
        fma8(acc, a[i], xs);
        PIN4(acc);
        SB();
        kk[i] = LDKV(rsK, i);
        SB();
      }
      STAMP(1);
      {
        LAS v4f* dst = (LAS v4f*)(act ? &L.part[qrc * 192 + qch * 8] : &L.dummy[0]);
        dst[0] = v4f{acc[0][0], acc[0][1], acc[1][0], acc[1][1]};
        dst[1] = v4f{acc[2][0], acc[2][1], acc[3][0], acc[3][1]};
      }
    }
    SB();
#pragma unroll
    for (int i = 8; i < 12; ++i) kk[i] = LDKV(rsK, i);
    SB();
    WG_BARRIER();
    SB();
#pragma unroll
    for (int i = 0; i < 6; ++i) vv[i] = LDKV(rsV, i);
    SB();
    STAMP(2);
    WG_BARRIER();
    STAMP(3);
    float sc[4];
    {
      const v4u qa = *(LAS v4u*)&L.qbf[4 * (lane & 15)];
      const v4u qb = *(LAS v4u*)&L.qbf[64 + 4 * (lane & 7)];
#pragma unroll
      for (int g = 0; g < 4; ++g) {
        float d1 = dot8(kk[3 * g], qa), d2 = dot8(kk[3 * g + 1], qa);
        const float d3 = dot8(kk[3 * g + 2], qb);
        d1 += dpp<DPP_ROR8>(d1);
        d2 += dpp<DPP_ROR8>(d2);
        float x = ((lane & 8) ? d2 : d1) + d3;
        x += dpp<DPP_XOR1>(x);
        x += dpp<DPP_XOR2>(x);
        x += dpp<DPP_HMIR>(x);
        sc[g] = rbf(rbf(x) * SCALE);
      }
    }
    {
      float m = fmaxf(fmaxf(sc[0], sc[1]), fmaxf(sc[2], sc[3]));
      m = fmaxf(m, dpp<DPP_ROR8>(m));
      m = max_rows(m);
      if (lane == 0) L.mw[w] = m;
    }
    SB();
#pragma unroll
    for (int i = 6; i < 12; ++i) vv[i] = LDKV(rsV, i);
    SB();
    WG_BARRIER();
    float pr[4];
    {
      const v4f m0 = *(LAS v4f*)&L.mw[0], m1 = *(LAS v4f*)&L.mw[4];
      const float m = fmaxf(fmaxf(fmaxf(m0[0], m0[1]), fmaxf(m0[2], m0[3])), fmaxf(fmaxf(m1[0], m1[1]), fmaxf(m1[2], m1[3])));
#pragma unroll
      for (int g = 0; g < 4; ++g) pr[g] = __builtin_amdgcn_exp2f((sc[g] - m) * LOG2E);
      float ls = (pr[0] + pr[1]) + (pr[2] + pr[3]);
      ls += dpp<DPP_ROR8>(ls);
      ls = sum_rows(ls);
      if (lane == 0) L.lw[w] = ls;
    }
    STAMP(4);
    const int orc = t / 40, och = t - orc * 40;
    const int voo = orc * 8 * (D * 2) + och * 16;
    v4u ww[8];
    {
      v2f a12[4] = {{0.f, 0.f}, {0.f, 0.f}, {0.f, 0.f}, {0.f, 0.f}}, a3[4] = {{0.f, 0.f}, {0.f, 0.f}, {0.f, 0.f}, {0.f, 0.f}};
#pragma unroll
      for (int g = 0; g < 4; ++g) {
        const float p1 = dpp<DPP_BC0>(pr[g]), p2 = dpp<DPP_BC8>(pr[g]);
        fma8(a12, vv[3 * g], p1);
        fma8(a12, vv[3 * g + 1], p2);
        fma8(a3, vv[3 * g + 2], pr[g]);
        PIN4(a12); PIN4(a3);
        SB();
        if (g < 3) {
          ww[2 * g] = LDNT(rsO, voo, (2 * g) * (D * 2));
          ww[2 * g + 1] = LDNT(rsO, voo, (2 * g + 1) * (D * 2));
        }
        SB();
      }
      STAMP(5);
#pragma unroll
      for (int i = 0; i < 4; ++i) { a3[i][0] += dpp<DPP_ROR8>(a3[i][0]); a3[i][1] += dpp<DPP_ROR8>(a3[i][1]); }
      const v2f c12 = fold_rows(a12), c3 = fold_rows(a3);
      const int r = lane >> 4, sub = 2 * (r & 1) + 4 * (r >> 1);
      *(LAS v2f*)&L.opart[w][8 * (lane & 15) + sub] = c12;
      if (!(lane & 8)) *(LAS v2f*)&L.opart[w][128 + 8 * (lane & 7) + sub] = c3;
    }
    WG_BARRIER();
    SB();
    ww[6] = LDNT(rsO, voo, 6 * (D * 2));
    ww[7] = LDNT(rsO, voo, 7 * (D * 2));
    SB();
    v2f wf[8][4];
#pragma unroll
    for (int i = 0; i < 8; ++i)
#pragma unroll
      for (int j = 0; j < 4; ++j) { wf[i][j] = v2f{bflo(ww[i][j]), bfhi(ww[i][j])}; asm volatile("" : "+v"(wf[i][j])); }
    STAMP(6);
    WG_BARRIER();
    STAMP(7);
    {
      const v4f o0 = *(LAS v4f*)&L.ofin[orc * 8], o1 = *(LAS v4f*)&L.ofin[orc * 8 + 4];
      v2f acc[4] = {{0.f, 0.f}, {0.f, 0.f}, {0.f, 0.f}, {0.f, 0.f}};
#pragma unroll
      for (int i = 0; i < 8; ++i) {
        const float os = i < 4 ? o0[i] : o1[i - 4];
        const v2f ss = {os, os};
#pragma unroll
        for (int j = 0; j < 4; ++j) acc[j] = __builtin_elementwise_fma(wf[i][j], ss, acc[j]);
      }
      {
        LAS v4f* dst = (LAS v4f*)(act ? &L.part[orc * 320 + och * 8] : &L.dummy[0]);
        dst[0] = v4f{acc[0][0], acc[0][1], acc[1][0], acc[1][1]};
        dst[1] = v4f{acc[2][0], acc[2][1], acc[3][0], acc[3][1]};
      }
    }
    WG_BARRIER();
    if (tid < 320) {
      float v = 0.f;
#pragma unroll
      for (int i = 0; i < 12; ++i) v += L.part[i * 320 + tid];
      const __amdgpu_buffer_rsrc_t rsW = __builtin_amdgcn_make_buffer_rsrc((void*)ws, 0, WS_USED, RSRC_W3);
      const u32 off = FX_OFF + (u32)((p * 2 + ch) * 40 + (tid >> 3)) * 4096 + (u32)(h * 2 + rh) * 64 + (tid & 7) * 8;
      __builtin_amdgcn_raw_buffer_store_b64(v2u{__float_as_uint(v), L.tag}, rsW, (int)off, 0, AUX_SC1);
    }
    STAMP(8);
  } else {
    const __amdgpu_buffer_rsrc_t rsW = __builtin_amdgcn_make_buffer_rsrc((void*)ws, 0, WS_USED, RSRC_W3);
    const __amdgpu_buffer_rsrc_t rsC = __builtin_amdgcn_make_buffer_rsrc((void*)(pc0 & ~0xFFull), 0, 8448, RSRC_W3);
    const u32 pf0 = __builtin_amdgcn_raw_buffer_load_b32(rsC, lane * 64, 0, 0);
    const u32 pf1 = __builtin_amdgcn_raw_buffer_load_b32(rsC, lane * 64, 4096, 0);
    const u32 pf2 = __builtin_amdgcn_raw_buffer_load_b32(rsC, lane * 64, 8192, 0);
    const u32 T = __builtin_amdgcn_readfirstlane(__builtin_amdgcn_raw_buffer_load_b32(rsW, p * 128, 0, AUX_SC1)) + 1u;
    if (lane == 0) L.tag = T;
    const int l48 = lane < 48 ? lane : 47;
    asm volatile("" :: "v"(pf0), "v"(pf1), "v"(pf2));
    WG_BARRIER();
    STAMP(1);
    {
      v4f q = {0.f, 0.f, 0.f, 0.f};
#pragma unroll
      for (int i = 0; i < 20; ++i) q += *(LAS v4f*)&L.part[i * 192 + 4 * l48];
      const u32 base = QX_OFF + (u32)((p * 32 + h) * 4) * QX_UNIT + l48 * 32;
      if (lane < 48) {
        __builtin_amdgcn_raw_buffer_store_b128(v4u{__float_as_uint(q[0]), T, __float_as_uint(q[1]), T}, rsW, (int)(base + s * QX_UNIT), 0, AUX_SC1);
        __builtin_amdgcn_raw_buffer_store_b128(v4u{__float_as_uint(q[2]), T, __float_as_uint(q[3]), T}, rsW, (int)(base + s * QX_UNIT + 16), 0, AUX_SC1);
      }
      STAMP(2);
      v4u g[4][2];
      for (u32 spin = 0; spin < SPIN_CAP; ++spin) {
        asm volatile("" ::: "memory");
        bool ok = true;
#pragma unroll
        for (int j = 0; j < 4; ++j) { g[j][0] = LDSC(rsW, base + j * QX_UNIT); g[j][1] = LDSC(rsW, base + j * QX_UNIT + 16); }
#pragma unroll
        for (int j = 0; j < 4; ++j) ok = ok & (g[j][0][1] == T) & (g[j][0][3] == T) & (g[j][1][1] == T) & (g[j][1][3] == T);
        if (__builtin_amdgcn_ballot_w64(!ok) == 0) break;
      }
      float qq[4];
#pragma unroll
      for (int e = 0; e < 4; ++e) {
        float v = __uint_as_float(g[0][e >> 1][(e & 1) * 2]);
#pragma unroll
        for (int j = 1; j < 4; ++j) v += __uint_as_float(g[j][e >> 1][(e & 1) * 2]);
        qq[e] = v;
      }
      if (lane < 48) *(LAS v2u*)&L.qbf[2 * lane] = v2u{bfbits(qq[0]) | (bfbits(qq[1]) << 16), bfbits(qq[2]) | (bfbits(qq[3]) << 16)};
    }
    STAMP(3);
    WG_BARRIER();
    WG_BARRIER();
    float m;
    {
      const v4f m0 = *(LAS v4f*)&L.mw[0], m1 = *(LAS v4f*)&L.mw[4];
      m = fmaxf(fmaxf(fmaxf(m0[0], m0[1]), fmaxf(m0[2], m0[3])), fmaxf(fmaxf(m1[0], m1[1]), fmaxf(m1[2], m1[3])));
    }
    WG_BARRIER();
    STAMP(4);
    {
      v4f o = {0.f, 0.f, 0.f, 0.f};
#pragma unroll
      for (int i = 0; i < 8; ++i) o += *(LAS v4f*)&L.opart[i][4 * l48];
      const v4f l0 = *(LAS v4f*)&L.lw[0], l1 = *(LAS v4f*)&L.lw[4];
      const float l = ((l0[0] + l0[1]) + (l0[2] + l0[3])) + ((l1[0] + l1[1]) + (l1[2] + l1[3]));
      const u32 base = OX_OFF + (u32)((p * 32 + h) * 4) * OX_UNIT;
      const bool isv = lane < 48;
      const v4u d0 = {__float_as_uint(isv ? o[0] : m), T, __float_as_uint(isv ? o[1] : l), T};
      if (lane <= 48) __builtin_amdgcn_raw_buffer_store_b128(d0, rsW, (int)(base + s * OX_UNIT + lane * 32), 0, AUX_SC1);
      if (lane < 48) __builtin_amdgcn_raw_buffer_store_b128(v4u{__float_as_uint(o[2]), T, __float_as_uint(o[3]), T}, rsW, (int)(base + s * OX_UNIT + lane * 32 + 16), 0, AUX_SC1);
      STAMP(5);
      const u32 roff = base + (isv ? (u32)(48 * rh + lane) * 16 : 1536u);
      v4u g[4];
      for (u32 spin = 0; spin < SPIN_CAP; ++spin) {
        asm volatile("" ::: "memory");
        bool ok = true;
#pragma unroll
        for (int j = 0; j < 4; ++j) g[j] = LDSC(rsW, roff + j * OX_UNIT);
#pragma unroll
        for (int j = 0; j < 4; ++j) ok = ok & (g[j][1] == T) & (g[j][3] == T);
        if (__builtin_amdgcn_ballot_w64(!ok) == 0) break;
      }
      float ms[4], ls[4];
#pragma unroll
      for (int j = 0; j < 4; ++j) {
        ms[j] = __uint_as_float(__builtin_amdgcn_readlane(g[j][0], 48));
        ls[j] = __uint_as_float(__builtin_amdgcn_readlane(g[j][2], 48));
      }
      const float M = fmaxf(fmaxf(ms[0], ms[1]), fmaxf(ms[2], ms[3]));
      float Ls = 0.f, o0 = 0.f, o1 = 0.f;
#pragma unroll
      for (int j = 0; j < 4; ++j) {
        const float f = __builtin_amdgcn_exp2f((ms[j] - M) * LOG2E);
        Ls += f * ls[j];
        o0 += f * __uint_as_float(g[j][0]);
        o1 += f * __uint_as_float(g[j][2]);
      }
      const float inv = 1.0f / Ls;
      if (lane < 48) *(LAS v2f*)&L.ofin[2 * lane] = v2f{rbf(o0 * inv), rbf(o1 * inv)};
    }
    STAMP(6);
    WG_BARRIER();
    WG_BARRIER();
    const int li = h * 4 + s;
    if (li < 80) {
      const u32 base = FX_OFF + (u32)(p * 80 + li) * 4096 + lane * 16;
      v4u g[4];
      STAMP(7);
      for (u32 spin = 0; spin < SPIN_CAP; ++spin) {
        asm volatile("" ::: "memory");
        bool ok = true;
#pragma unroll
        for (int j = 0; j < 4; ++j) g[j] = LDSC(rsW, base + j * 1024);
#pragma unroll
        for (int j = 0; j < 4; ++j) ok = ok & (g[j][1] == T) & (g[j][3] == T);
        if (__builtin_amdgcn_ballot_w64(!ok) == 0) break;
      }
      float v0 = (__uint_as_float(g[0][0]) + __uint_as_float(g[1][0])) + (__uint_as_float(g[2][0]) + __uint_as_float(g[3][0]));
      float v1 = (__uint_as_float(g[0][2]) + __uint_as_float(g[1][2])) + (__uint_as_float(g[2][2]) + __uint_as_float(g[3][2]));
      v0 += dpp<DPP_ROR8>(v0); v1 += dpp<DPP_ROR8>(v1);
      v0 += dpp<DPP_ROR4>(v0); v1 += dpp<DPP_ROR4>(v1);
      v0 = sum_rows(v0); v1 = sum_rows(v1);
      if (lane < 4) ((u32*)out)[(p * D + li * 8) / 2 + lane] = bfbits(v0) | (bfbits(v1) << 16);
      if (li == 0 && lane == 0) __builtin_amdgcn_raw_buffer_store_b32(T, rsW, p * 128, 0, AUX_SC1);
      STAMP(8);
    }
  }
}
}

extern "C" void kernel_launch(void* const* d_in, const int* in_sizes, int n_in, void* d_out, int out_size, void* d_ws, size_t ws_size,
                              hipStream_t stream) {
  fused_decode<<<256, NTHR, 0, stream>>>((const u16*)d_in[0], (const u16*)d_in[1], (const u16*)d_in[2], (const u16*)d_in[3], (const u16*)d_in[4],
                                         (const u16*)d_in[5], (const u16*)d_in[6], (const u16*)d_in[7], (const u16*)d_in[8], (const u16*)d_in[9],
                                         (u16*)d_out, (unsigned char*)d_ws);
}
```

```cpp
#include <hip/hip_runtime.h>

typedef unsigned short u16;
typedef unsigned int u32;
typedef u32 v4u __attribute__((ext_vector_type(4)));
typedef u32 v2u __attribute__((ext_vector_type(2)));
typedef float v2f __attribute__((ext_vector_type(2)));
typedef float v4f __attribute__((ext_vector_type(4)));
typedef __bf16 v2bf __attribute__((ext_vector_type(2)));
#define LAS __attribute__((address_space(3)))

#define WN 5
#define QW 22

namespace {
constexpr int D = 640, H = 32, SK = 4096, DH = 192, W = 1024, HD = H * DH;
constexpr int NTHR = 512;
constexpr float SCALE = 0.07216878364870322f;
constexpr float LOG2E = 1.4426950408889634f;
constexpr u32 SX_OFF = 4096;
constexpr u32 FX_OFF = SX_OFF + 64 * 3 * 8 * 1024;
constexpr u32 WS_USED = FX_OFF + 2 * 80 * 6144;
constexpr int RSRC_W3 = 0x00020000;
constexpr int AUX_NT = 2, AUX_SC1 = 16;
constexpr u32 SPIN_CAP = 1u << 16;

struct alignas(16) Lds {
  float part[8][640];
  float qpart[8][64];
  float opart[8][64];
  float pbuf[8][128];
  float ml[8][2];
  u32 qbf[32];
  u32 tag, cntq, qflag, pad;
};

__device__ __forceinline__ float bflo(u32 u) { return __uint_as_float(u << 16); }
__device__ __forceinline__ float bfhi(u32 u) { return __uint_as_float(u & 0xffff0000u); }
__device__ __forceinline__ u32 bfbits(float f) { return (u32)__builtin_bit_cast(u16, (__bf16)f); }
__device__ __forceinline__ float rbf(float f) { return __uint_as_float(bfbits(f) << 16); }
template <int CTRL> __device__ __forceinline__ float dpp(float x) {
  return __int_as_float(__builtin_amdgcn_update_dpp(0, __float_as_int(x), CTRL, 0xf, 0xf, true));
}
constexpr int DPP_XOR1 = 0xB1, DPP_XOR2 = 0x4E, DPP_HMIR = 0x141, DPP_ROR4 = 0x124, DPP_ROR8 = 0x128;
__device__ __forceinline__ float sum_rows(float x) {
  auto a = __builtin_amdgcn_permlane16_swap(__float_as_uint(x), __float_as_uint(x), false, false);
  x = __uint_as_float(a[0]) + __uint_as_float(a[1]);
  auto b = __builtin_amdgcn_permlane32_swap(__float_as_uint(x), __float_as_uint(x), false, false);
  return __uint_as_float(b[0]) + __uint_as_float(b[1]);
}
__device__ __forceinline__ float max_rows(float x) {
  auto a = __builtin_amdgcn_permlane16_swap(__float_as_uint(x), __float_as_uint(x), false, false);
  x = fmaxf(__uint_as_float(a[0]), __uint_as_float(a[1]));
  auto b = __builtin_amdgcn_permlane32_swap(__float_as_uint(x), __float_as_uint(x), false, false);
  return fmaxf(__uint_as_float(b[0]), __uint_as_float(b[1]));
}
__device__ __forceinline__ float dot8(v4u a, v4u b) {
  float acc = 0.f;
#pragma unroll
  for (int i = 0; i < 4; ++i) { const u32 x = a[i], y = b[i]; acc = __builtin_amdgcn_fdot2_f32_bf16(__builtin_bit_cast(v2bf, x), __builtin_bit_cast(v2bf, y), acc, false); }
  return acc;
}
__device__ __forceinline__ void fma8(v2f (&acc)[4], v4u wv, float s) {
  const v2f ss = {s, s};
#pragma unroll
  for (int i = 0; i < 4; ++i) { const v2f u = {bflo(wv[i]), bfhi(wv[i])}; acc[i] = __builtin_elementwise_fma(u, ss, acc[i]); }
}
__device__ __forceinline__ v2f fold_rows(const v2f (&a)[4]) {
  float b[4];
#pragma unroll
  for (int j = 0; j < 4; ++j) {
    const float lo = j < 2 ? a[0][j] : a[1][j - 2], hi = j < 2 ? a[2][j] : a[3][j - 2];
    auto r = __builtin_amdgcn_permlane32_swap(__float_as_uint(lo), __float_as_uint(hi), false, false);
    b[j] = __uint_as_float(r[0]) + __uint_as_float(r[1]);
  }
  v2f c;
#pragma unroll
  for (int j = 0; j < 2; ++j) {
    auto r = __builtin_amdgcn_permlane16_swap(__float_as_uint(b[j]), __float_as_uint(b[j + 2]), false, false);
    c[j] = __uint_as_float(r[0]) + __uint_as_float(r[1]);
  }
  return c;
}
__device__ __forceinline__ float fold8(const v2f (&a)[4], bool b3) {
  const v2f c = fold_rows(a);
  const float keep = b3 ? c[1] : c[0], send = b3 ? c[0] : c[1];
  return keep + dpp<DPP_ROR8>(send);
}
constexpr int vmenc(int n) { return (n & 15) | (7 << 4) | (15 << 8) | ((n >> 4) << 14); }
#define SB() __builtin_amdgcn_sched_barrier(0)
#define PIN4(a) asm volatile("" : "+v"(a[0]), "+v"(a[1]), "+v"(a[2]), "+v"(a[3]))
#define WG_BARRIER() asm volatile("s_waitcnt lgkmcnt(0)\n\ts_barrier" ::: "memory")
#define LDNT(rs, vo, so) __builtin_amdgcn_raw_buffer_load_b128(rs, (int)(vo), (int)(so), AUX_NT)
#define LDSC(rs, vo) __builtin_amdgcn_raw_buffer_load_b128(rs, (int)(vo), 0, AUX_SC1)
#define VMWAIT(n) __builtin_amdgcn_s_waitcnt(vmenc(n))

#define STAMP(i) do {} while (0)

__global__ void __launch_bounds__(NTHR, 2)
fused_decode(const u16* __restrict__ xA, const u16* __restrict__ kA, const u16* __restrict__ vA, const u16* __restrict__ wqA, const u16* __restrict__ woA,
             const u16* __restrict__ xB, const u16* __restrict__ kB, const u16* __restrict__ vB, const u16* __restrict__ wqB, const u16* __restrict__ woB,
             u16* __restrict__ out, unsigned char* __restrict__ ws) {
  __shared__ Lds L;
  const int b = blockIdx.x, tid = threadIdx.x, lane = tid & 63, w = __builtin_amdgcn_readfirstlane(tid >> 6);
  const int hp = (b & 7) + 8 * (b / 24), j = (b >> 3) % 3;
  const int p = hp >> 5, h = hp & 31, li = h * 3 + j;
  const int kr = lane >> 3, c8 = lane & 7;
  const __amdgpu_buffer_rsrc_t rsQ = __builtin_amdgcn_make_buffer_rsrc((void*)((p ? wqB : wqA) + (size_t)(w * 80) * HD + h * DH + j * 64), 0, 79 * HD * 2 + 128, RSRC_W3);
  const __amdgpu_buffer_rsrc_t rsK = __builtin_amdgcn_make_buffer_rsrc((void*)((p ? kB : kA) + ((size_t)h * SK + (SK - W) + w * 128) * DH + j * 64), 0, 127 * DH * 2 + 128, RSRC_W3);
  const __amdgpu_buffer_rsrc_t rsX = __builtin_amdgcn_make_buffer_rsrc((void*)(p ? xB : xA), 0, D * 2, RSRC_W3);
  const __amdgpu_buffer_rsrc_t rsW = __builtin_amdgcn_make_buffer_rsrc((void*)ws, 0, WS_USED, RSRC_W3);
  const int voq = kr * 10 * (HD * 2) + c8 * 16;
  const int vok = kr * (DH * 2) + c8 * 16;
  const int voo = (lane >> 4) * (D * 2) + (lane & 15) * 16;
  v4u a[10], kk[16], vv[16], ww[10];
  a[0] = LDNT(rsQ, voq, 0);
  kk[0] = LDNT(rsK, vok, 0);
  const v4u xr = __builtin_amdgcn_raw_buffer_load_b128(rsX, (w * 80 + kr * 10) * 2, 0, 0);
  const u32 xr2 = __builtin_amdgcn_raw_buffer_load_b32(rsX, (w * 80 + kr * 10) * 2 + 16, 0, 0);
  const u32 Tv = __builtin_amdgcn_raw_buffer_load_b32(rsW, w == 0 ? p * 128 : 0x7ffffff0, 0, AUX_SC1);
  const __amdgpu_buffer_rsrc_t rsV = __builtin_amdgcn_make_buffer_rsrc((void*)((p ? vB : vA) + ((size_t)h * SK + (SK - W) + w * 128) * DH + j * 64), 0, 127 * DH * 2 + 128, RSRC_W3);
  const __amdgpu_buffer_rsrc_t rsO = __builtin_amdgcn_make_buffer_rsrc((void*)((p ? woB : woA) + ((size_t)h * DH + j * 64 + w * 8) * D), 0, 8 * D * 2, RSRC_W3);
#define ISSUE(n) do { \
    if ((n) < 20 && ((n) & 1) == 0) a[(n) < 20 ? (n) / 2 : 0] = LDNT(rsQ, voq, ((n) / 2) * (HD * 2)); \
    else if ((n) < 20) kk[(n) < 20 ? (n) / 2 : 0] = LDNT(rsK, vok, ((n) / 2) * 3072); \
    else if ((n) < 26) kk[(n) >= 20 && (n) < 26 ? (n) - 10 : 0] = LDNT(rsK, vok, ((n) - 10) * 3072); \
    else if ((n) < 42) vv[(n) >= 26 && (n) < 42 ? (n) - 26 : 0] = LDNT(rsV, vok, ((n) - 26) * 3072); \
    else if ((n) < 52) ww[(n) >= 42 && (n) < 52 ? (n) - 42 : 0] = LDNT(rsO, voo, (((n) - 42) % 5) * 256 + (((n) - 42) / 5) * 5120); \
  } while (0)
#pragma unroll
  for (int n = 2; n < WN; ++n) ISSUE(n);
  SB();
  if (tid == 0) { L.cntq = 0; L.qflag = 0; }
  WG_BARRIER();
  const bool b0 = lane & 1, b1 = lane & 2, b2 = lane & 4, b3 = lane & 8;
  {
    v2f acc[4] = {{0.f, 0.f}, {0.f, 0.f}, {0.f, 0.f}, {0.f, 0.f}};
#pragma unroll
    for (int n = 0; n <= 18; ++n) {
      if ((n & 1) == 0) {
        const int i = n / 2;
        const u32 xw = i < 8 ? xr[i >> 1] : xr2;
        fma8(acc, a[i], (i & 1) ? bfhi(xw) : bflo(xw));
        PIN4(acc);
      } else {
        VMWAIT(WN - 1);
      }
      SB();
      ISSUE(n + WN);
      SB();
    }
    STAMP(1);
    const float qp = fold8(acc, b3);
    L.qpart[w][c8 * 8 + (b3 ? 1 : 0) + 2 * ((lane >> 4) & 1) + 4 * (lane >> 5)] = qp;
    if (w == 0 && lane == 0) L.tag = Tv + 1u;
    asm volatile("" ::: "memory");
    u32 old = 0;
    if (lane == 0) old = __hip_atomic_fetch_add((LAS u32*)&L.cntq, 1u, __ATOMIC_RELAXED, __HIP_MEMORY_SCOPE_WORKGROUP);
    old = __builtin_amdgcn_readfirstlane(old);
    asm volatile("" ::: "memory");
    if (old == 7u) {
      float q = 0.f;
#pragma unroll
      for (int i = 0; i < 8; ++i) q += *(volatile LAS float*)&L.qpart[i][lane];
      ((LAS u16*)&L.qbf[0])[lane] = (u16)bfbits(q);
      asm volatile("" ::: "memory");
      if (lane == 0) *(volatile LAS u32*)&L.qflag = 1u;
      asm volatile("" ::: "memory");
    }
  }
  SB();
#pragma unroll
  for (int n = 19; n <= QW; ++n) {
    VMWAIT(WN - 1);
    SB();
    ISSUE(n + WN);
    SB();
  }
  STAMP(2);
  for (;;) {
    asm volatile("" ::: "memory");
    const u32 v = __builtin_amdgcn_readfirstlane(*(volatile LAS u32*)&L.qflag);
    if (v) break;
    __builtin_amdgcn_s_sleep(1);
  }
  asm volatile("" ::: "memory");
  const u32 T = __builtin_amdgcn_readfirstlane(*(volatile LAS u32*)&L.tag);
  const u32 sxh = SX_OFF + (u32)hp * (3 * 8192) + (u32)w * 1024 + lane * 16;
  float s0, s1;
  {
    const v4u qa = *(LAS v4u*)&L.qbf[4 * c8];
    STAMP(3);
    float d[16];
#pragma unroll
    for (int i = 0; i < 16; ++i) {
      d[i] = dot8(kk[i], qa);
      if (i + 10 > QW) {
        asm volatile("" : "+v"(d[i]));
        SB();
        ISSUE(i + 10 + WN);
        SB();
      }
    }
    float e[8], f[4];
#pragma unroll
    for (int i = 0; i < 8; ++i) { const float keep = b2 ? d[i + 8] : d[i], send = b2 ? d[i] : d[i + 8]; e[i] = keep + dpp<DPP_HMIR>(send); }
#pragma unroll
    for (int i = 0; i < 4; ++i) { const float keep = b0 ? e[i + 4] : e[i], send = b0 ? e[i] : e[i + 4]; f[i] = keep + dpp<DPP_XOR1>(send); }
    { const float keep = b1 ? f[2] : f[0], send = b1 ? f[0] : f[2]; s0 = keep + dpp<DPP_XOR2>(send); }
    { const float keep = b1 ? f[3] : f[1], send = b1 ? f[1] : f[3]; s1 = keep + dpp<DPP_XOR2>(send); }
    __builtin_amdgcn_raw_buffer_store_b128(v4u{__float_as_uint(s0), T, __float_as_uint(s1), T}, rsW, (int)(sxh + j * 8192), 0, AUX_SC1);
  }
  STAMP(4);
  SB();
  v2f vf[16][4];
  const u32 o1 = sxh + (j == 0 ? 1 : 0) * 8192, o2 = sxh + (j == 2 ? 1 : 2) * 8192;
  v4u g1, g2;
#pragma unroll
  for (int i = 0; i < 16; ++i) {
#pragma unroll
    for (int k = 0; k < 4; ++k) vf[i][k] = v2f{bflo(vv[i][k]), bfhi(vv[i][k])};
    PIN4(vf[i]);
    SB();
    ISSUE(26 + i + WN);
    if (26 + i + WN == 41) { g1 = LDSC(rsW, o1); g2 = LDSC(rsW, o2); }
    SB();
  }
  STAMP(5);
  if (__builtin_expect(__builtin_amdgcn_ballot_w64(!((g1[1] == T) & (g1[3] == T) & (g2[1] == T) & (g2[3] == T))) != 0, 0)) {
    for (u32 spin = 0; spin < SPIN_CAP; ++spin) {
      asm volatile("" ::: "memory");
      g1 = LDSC(rsW, o1); g2 = LDSC(rsW, o2);
      const bool ok = (g1[1] == T) & (g1[3] == T) & (g2[1] == T) & (g2[3] == T);
      if (__builtin_amdgcn_ballot_w64(!ok) == 0) break;
    }
  }
  STAMP(6);
  SB();
#pragma unroll
  for (int n = 42 + WN; n < 52; ++n) ISSUE(n);
  SB();
  {
    const float x0 = j == 0 ? s0 : __uint_as_float(g1[0]), x1 = j == 1 ? s0 : __uint_as_float(j == 0 ? g1[0] : g2[0]), x2 = j == 2 ? s0 : __uint_as_float(g2[0]);
    const float y0 = j == 0 ? s1 : __uint_as_float(g1[2]), y1 = j == 1 ? s1 : __uint_as_float(j == 0 ? g1[2] : g2[2]), y2 = j == 2 ? s1 : __uint_as_float(g2[2]);
    const float sa = rbf(rbf((x0 + x1) + x2) * SCALE), sb = rbf(rbf((y0 + y1) + y2) * SCALE);
    float m = fmaxf(sa, sb);
    m = fmaxf(m, dpp<DPP_XOR1>(m));
    m = fmaxf(m, dpp<DPP_XOR2>(m));
    m = fmaxf(m, dpp<DPP_HMIR>(m));
    m = fmaxf(m, dpp<DPP_ROR8>(m));
    m = max_rows(m);
    const float pa = __builtin_amdgcn_exp2f((sa - m) * LOG2E), pb = __builtin_amdgcn_exp2f((sb - m) * LOG2E);
    *(LAS v2f*)&L.pbuf[w][kr * 16 + (b2 ? 8 : 0) + (b0 ? 4 : 0) + (b1 ? 2 : 0)] = v2f{pa, pb};
    asm volatile("" ::: "memory");
    v4f pv[4];
#pragma unroll
    for (int k = 0; k < 4; ++k) pv[k] = *(LAS v4f*)&L.pbuf[w][kr * 16 + 4 * k];
    float ls = pa + pb;
    ls += dpp<DPP_XOR1>(ls);
    ls += dpp<DPP_XOR2>(ls);
    ls += dpp<DPP_HMIR>(ls);
    ls += dpp<DPP_ROR8>(ls);
    ls = sum_rows(ls);
    v2f acc[4] = {{0.f, 0.f}, {0.f, 0.f}, {0.f, 0.f}, {0.f, 0.f}};
#pragma unroll
    for (int i = 0; i < 16; ++i) {
      const float ps = pv[i >> 2][i & 3];
      const v2f ss = {ps, ps};
#pragma unroll
      for (int k = 0; k < 4; ++k) acc[k] = __builtin_elementwise_fma(vf[i][k], ss, acc[k]);
    }
    const float op = fold8(acc, b3);
    L.opart[w][c8 * 8 + (b3 ? 1 : 0) + 2 * ((lane >> 4) & 1) + 4 * (lane >> 5)] = op;
    if (lane == 0) *(LAS v2f*)&L.ml[w][0] = v2f{m, ls};
  }
  STAMP(7);
  WG_BARRIER();
  {
    const int r = (lane >> 4) + (b3 ? 4 : 0);
    const float ov = L.opart[c8][w * 8 + r];
    const v2f ml = *(LAS v2f*)&L.ml[c8][0];
    float M = ml[0];
    M = fmaxf(M, dpp<DPP_XOR1>(M));
    M = fmaxf(M, dpp<DPP_XOR2>(M));
    M = fmaxf(M, dpp<DPP_HMIR>(M));
    const float f = __builtin_amdgcn_exp2f((ml[0] - M) * LOG2E);
    float on = f * ov, ln = f * ml[1];
    on += dpp<DPP_XOR1>(on); ln += dpp<DPP_XOR1>(ln);
    on += dpp<DPP_XOR2>(on); ln += dpp<DPP_XOR2>(ln);
    on += dpp<DPP_HMIR>(on); ln += dpp<DPP_HMIR>(ln);
    const float o = rbf(on / ln);
    const float ox = dpp<DPP_ROR8>(o);
    const float og0 = b3 ? ox : o, og1 = b3 ? o : ox;
    STAMP(8);
    v2f acc[5][4];
#pragma unroll
    for (int cb = 0; cb < 5; ++cb) {
#pragma unroll
      for (int k = 0; k < 4; ++k) acc[cb][k] = v2f{0.f, 0.f};
      fma8(acc[cb], ww[cb], og0);
    }
#pragma unroll
    for (int cb = 0; cb < 5; ++cb) {
      fma8(acc[cb], ww[5 + cb], og1);
      const v2f c = fold_rows(acc[cb]);
      const int rr = lane >> 4;
      *(LAS v2f*)&L.part[w][cb * 128 + 8 * (lane & 15) + 2 * (rr & 1) + 4 * (rr >> 1)] = c;
    }
  }
  STAMP(10);
  WG_BARRIER();
  if (tid < 320) {
    v2f v = {0.f, 0.f};
#pragma unroll
    for (int i = 0; i < 8; ++i) v += *(LAS v2f*)&L.part[i][2 * tid];
    const u32 off = FX_OFF + (u32)((p * 80 + (tid >> 2)) * 96 + li) * 64 + (tid & 3) * 16;
    __builtin_amdgcn_raw_buffer_store_b128(v4u{__float_as_uint(v[0]), T, __float_as_uint(v[1]), T}, rsW, (int)off, 0, AUX_SC1);
    STAMP(11);
  } else if (w == 7 && li < 80) {
    const u32 base = FX_OFF + (u32)(p * 80 + li) * 6144 + lane * 16;
    v4u g[6];
    for (u32 spin = 0; spin < SPIN_CAP; ++spin) {
      asm volatile("" ::: "memory");
      bool ok = true;
#pragma unroll
      for (int k = 0; k < 6; ++k) g[k] = LDSC(rsW, base + k * 1024);
#pragma unroll
      for (int k = 0; k < 6; ++k) ok = ok & (g[k][1] == T) & (g[k][3] == T);
      if (__builtin_amdgcn_ballot_w64(!ok) == 0) break;
    }
    float v0 = 0.f, v1 = 0.f;
#pragma unroll
    for (int k = 0; k < 6; ++k) { v0 += __uint_as_float(g[k][0]); v1 += __uint_as_float(g[k][2]); }
    v0 += dpp<DPP_ROR8>(v0); v1 += dpp<DPP_ROR8>(v1);
    v0 += dpp<DPP_ROR4>(v0); v1 += dpp<DPP_ROR4>(v1);
    v0 = sum_rows(v0); v1 = sum_rows(v1);
    if (lane < 4) ((u32*)out)[(p * D + li * 8) / 2 + lane] = bfbits(v0) | (bfbits(v1) << 16);
    if (li == 0 && lane == 0) __builtin_amdgcn_raw_buffer_store_b32(T, rsW, p * 128, 0, AUX_SC1);
    STAMP(12);
  }
}
}

extern "C" void kernel_launch(void* const* d_in, const int* in_sizes, int n_in, void* d_out, int out_size, void* d_ws, size_t ws_size,
                              hipStream_t stream) {
  fused_decode<<<192, NTHR, 0, stream>>>((const u16*)d_in[0], (const u16*)d_in[1], (const u16*)d_in[2], (const u16*)d_in[3], (const u16*)d_in[4],
                                         (const u16*)d_in[5], (const u16*)d_in[6], (const u16*)d_in[7], (const u16*)d_in[8], (const u16*)d_in[9],
                                         (u16*)d_out, (unsigned char*)d_ws);
}
```

```cpp
#include <hip/hip_runtime.h>

typedef unsigned short u16;
typedef unsigned int u32;
typedef u32 v4u __attribute__((ext_vector_type(4)));
typedef u32 v2u __attribute__((ext_vector_type(2)));
typedef float v2f __attribute__((ext_vector_type(2)));
typedef float v4f __attribute__((ext_vector_type(4)));
typedef __bf16 v2bf __attribute__((ext_vector_type(2)));
typedef __bf16 v8bf __attribute__((ext_vector_type(8)));
#define LAS __attribute__((address_space(3)))
#define CAS __attribute__((address_space(4)))

#define PSL 7

namespace {
constexpr int D = 640, H = 32, SK = 4096, DH = 192, W = 1024, HD = H * DH;
constexpr int NTHR = 512;
constexpr float SCALE = 0.07216878364870322f;
constexpr float LOG2E = 1.4426950408889634f;
constexpr u32 SX_OFF = 4096;
constexpr u32 FX_OFF = SX_OFF + 64 * 3 * 8 * 1024;
constexpr u32 WS_USED = FX_OFF + 2 * 80 * 6144;
constexpr int RSRC_W3 = 0x00020000;
constexpr int AUX_NT = 2, AUX_SC1 = 16;
constexpr u32 SPIN_CAP = 1u << 16;

struct alignas(16) Lds {
  float qp[16][64];
  float opart[8][2][64];
  u32 pbuf[8][128];
  float ml[8][2];
  u32 qbf[32];
  u32 cnt[8];
};

__device__ __forceinline__ float bflo(u32 u) { return __uint_as_float(u << 16); }
__device__ __forceinline__ float bfhi(u32 u) { return __uint_as_float(u & 0xffff0000u); }
__device__ __forceinline__ u32 bfbits(float f) { return (u32)__builtin_bit_cast(u16, (__bf16)f); }
__device__ __forceinline__ float rbf(float f) { return __uint_as_float(bfbits(f) << 16); }
__device__ __forceinline__ u32 dup_bf(float f) { const v2bf r = {(__bf16)f, (__bf16)f}; return __builtin_bit_cast(u32, r); }
template <int CTRL> __device__ __forceinline__ float dpp(float x) {
  return __int_as_float(__builtin_amdgcn_update_dpp(0, __float_as_int(x), CTRL, 0xf, 0xf, true));
}
template <int CTRL> __device__ __forceinline__ u32 dppu(u32 x) { return (u32)__builtin_amdgcn_update_dpp(0, (int)x, CTRL, 0xf, 0xf, true); }
constexpr int DPP_XOR1 = 0xB1, DPP_XOR2 = 0x4E, DPP_HMIR = 0x141, DPP_ROR4 = 0x124, DPP_ROR8 = 0x128, DPP_BCAST = 0x150;
__device__ __forceinline__ float sum_rows(float x) {
  auto a = __builtin_amdgcn_permlane16_swap(__float_as_uint(x), __float_as_uint(x), false, false);
  x = __uint_as_float(a[0]) + __uint_as_float(a[1]);
  auto b = __builtin_amdgcn_permlane32_swap(__float_as_uint(x), __float_as_uint(x), false, false);
  return __uint_as_float(b[0]) + __uint_as_float(b[1]);
}
__device__ __forceinline__ float max_rows(float x) {
  auto a = __builtin_amdgcn_permlane16_swap(__float_as_uint(x), __float_as_uint(x), false, false);
  x = fmaxf(__uint_as_float(a[0]), __uint_as_float(a[1]));
  auto b = __builtin_amdgcn_permlane32_swap(__float_as_uint(x), __float_as_uint(x), false, false);
  return fmaxf(__uint_as_float(b[0]), __uint_as_float(b[1]));
}
__device__ __forceinline__ float dot8(v4u a, v4u b) {
  float acc = 0.f;
#pragma unroll
  for (int i = 0; i < 4; ++i) { const u32 x = a[i], y = b[i]; acc = __builtin_amdgcn_fdot2_f32_bf16(__builtin_bit_cast(v2bf, x), __builtin_bit_cast(v2bf, y), acc, false); }
  return acc;
}
__device__ __forceinline__ v4f sel_mfma(u32 cp, v4u mk, v4u data, v4f acc) {
  const v4u a = {cp & mk[0], cp & mk[1], cp & mk[2], cp & mk[3]};
  return __builtin_amdgcn_mfma_f32_16x16x32_bf16(__builtin_bit_cast(v8bf, a), __builtin_bit_cast(v8bf, data), acc, 0, 0, 0);
}
template <int I> __device__ __forceinline__ u32 bcast8(u32 x) {
  const int t = __builtin_amdgcn_update_dpp(0, (int)x, DPP_BCAST + I, 0xf, 0x3, false);
  return (u32)__builtin_amdgcn_update_dpp(t, (int)x, DPP_BCAST + 8 + I, 0xf, 0xc, false);
}
__device__ __forceinline__ u32 lds_inc(LAS u32* c, int lane) {
  asm volatile("" ::: "memory");
  u32 old = 0;
  if (lane == 0) old = __hip_atomic_fetch_add(c, 1u, __ATOMIC_RELAXED, __HIP_MEMORY_SCOPE_WORKGROUP);
  old = __builtin_amdgcn_readfirstlane(old);
  asm volatile("" ::: "memory");
  return old;
}
__device__ __forceinline__ void lds_wait(LAS u32* c, u32 n) {
  for (;;) {
    asm volatile("" ::: "memory");
    const u32 v = __builtin_amdgcn_readfirstlane(*(volatile LAS u32*)c);
    if (v >= n) break;
    __builtin_amdgcn_s_sleep(1);
  }
  asm volatile("" ::: "memory");
}
constexpr int vmenc(int n) { return (n & 15) | (7 << 4) | (15 << 8) | ((n >> 4) << 14); }
#define SB() __builtin_amdgcn_sched_barrier(0)
#define LDNT(rs, vo, so) __builtin_amdgcn_raw_buffer_load_b128(rs, (int)(vo), (int)(so), AUX_NT)
#define LDSC(rs, vo) __builtin_amdgcn_raw_buffer_load_b128(rs, (int)(vo), 0, AUX_SC1)
#define VMWAIT(n) __builtin_amdgcn_s_waitcnt(vmenc(n))

#define STAMP(i) do {} while (0)
#define STAMP_FLUSH() do {} while (0)

__global__ void __launch_bounds__(NTHR, 2)
fused_decode(const u16* __restrict__ xA, const u16* __restrict__ kA, const u16* __restrict__ vA, const u16* __restrict__ wqA, const u16* __restrict__ woA,
             const u16* __restrict__ xB, const u16* __restrict__ kB, const u16* __restrict__ vB, const u16* __restrict__ wqB, const u16* __restrict__ woB,
             u16* __restrict__ out, unsigned char* ws) {
  __shared__ Lds L;
  const int b = blockIdx.x, tid = threadIdx.x, lane = tid & 63, w = __builtin_amdgcn_readfirstlane(tid >> 6);
  const int p = (b >> 3) >= 12 ? 1 : 0, li = 12 * (b & 7) + (b >> 3) - 12 * p, h = li / 3, j = li - 3 * h;
  const int hp = p * 32 + h;
  const int kr = lane >> 3, c8 = lane & 7;
  const u32 tstart = (u32)__builtin_amdgcn_s_memrealtime();
  u32 lvl = 0;
  unsigned long long tnow;
#define AWAIT(n2, n1, n0) asm volatile("s_waitcnt vmcnt(%1)\n s_cmp_gt_u32 %0, 1\n s_cbranch_scc1 1f\n s_waitcnt vmcnt(%2)\n s_cmp_gt_u32 %0, 0\n s_cbranch_scc1 1f\n s_waitcnt vmcnt(%3)\n1:" :: "s"(lvl), "n"(n2), "n"(n1), "n"(n0) : "scc", "memory")
  constexpr int QSTEP = 64;
  const __amdgpu_buffer_rsrc_t rsQ = __builtin_amdgcn_make_buffer_rsrc((void*)((p ? wqB : wqA) + (size_t)(w * 8) * HD + h * DH + j * 64), 0, (576 + 7) * HD * 2 + 128, RSRC_W3);
  const int voq = kr * (HD * 2) + c8 * 16;
#define XOFF(i) (2 * (64 * (i) + 8 * w + kr))
  const __amdgpu_buffer_rsrc_t rsW = __builtin_amdgcn_make_buffer_rsrc((void*)ws, 0, WS_USED, RSRC_W3);
  const int vok = kr * (DH * 2) + c8 * 16;
  const int voo = (lane >> 4) * (D * 2) + (lane & 15) * 16;
  v4u a[10], kk[16], vv[16], ww[16];
  asm volatile("" :: "s"(xA), "s"(kA), "s"(vA), "s"(woA), "s"(xB), "s"(kB), "s"(vB), "s"(woB), "s"(out), "s"(ws));
  const __amdgpu_buffer_rsrc_t rsX = __builtin_amdgcn_make_buffer_rsrc((void*)(p ? xB : xA), 0, D * 2, RSRC_W3);
  u32 xv0 = __builtin_amdgcn_raw_buffer_load_b16(rsX, XOFF(c8), 0, 0), xv1 = __builtin_amdgcn_raw_buffer_load_b16(rsX, XOFF(8 + (c8 & 1)), 0, 0);
  SB(); a[0] = LDNT(rsQ, voq, 0 * (QSTEP * HD * 2)); SB();
  asm volatile("s_barrier" ::: "memory");
  SB(); a[1] = LDNT(rsQ, voq, 1 * (QSTEP * HD * 2)); SB();
  if (tid == 0) { *(LAS v4u*)&L.cnt[0] = v4u{0u, 0u, 0u, 0u}; *(LAS v4u*)&L.cnt[4] = v4u{0u, 0u, 0u, 0u}; }
  asm volatile("s_waitcnt lgkmcnt(0)" ::: "memory");
  SB();
  u32 T = *(const CAS u32*)(ws + p * 128);
  const __amdgpu_buffer_rsrc_t rsK = __builtin_amdgcn_make_buffer_rsrc((void*)((p ? kB : kA) + ((size_t)h * SK + (SK - W) + w * 128) * DH + j * 64), 0, 127 * DH * 2 + 128, RSRC_W3);
  const __amdgpu_buffer_rsrc_t rsV = __builtin_amdgcn_make_buffer_rsrc((void*)((p ? vB : vA) + ((size_t)h * SK + (SK - W) + w * 128) * DH + j * 64), 0, 127 * DH * 2 + 128, RSRC_W3);
  const __amdgpu_buffer_rsrc_t rsO = __builtin_amdgcn_make_buffer_rsrc((void*)((p ? woB : woA) + ((size_t)h * DH + j * 64) * D + (w >= 3 ? w - 3 : 0) * 128), 0, w >= 3 ? 63 * D * 2 + 256 : 0, RSRC_W3);
  asm volatile("s_barrier" ::: "memory");
  SB(); a[2] = LDNT(rsQ, voq, 2 * (QSTEP * HD * 2)); SB();
  asm volatile("s_barrier" ::: "memory");
  SB(); a[3] = LDNT(rsQ, voq, 3 * (QSTEP * HD * 2)); SB();
  asm volatile("s_barrier" ::: "memory");
  SB(); a[4] = LDNT(rsQ, voq, 4 * (QSTEP * HD * 2)); SB();
  STAMP(9);
  const bool b0 = lane & 1, b1 = lane & 2, b2 = lane & 4;
  const u32 mkv = (lane & 1) ? 0xffff0000u : 0x0000ffffu;
  const v4u mk = {(c8 >> 1) == 0 ? mkv : 0u, (c8 >> 1) == 1 ? mkv : 0u, (c8 >> 1) == 2 ? mkv : 0u, (c8 >> 1) == 3 ? mkv : 0u};
  const u32 sxh = SX_OFF + (u32)hp * (3 * 8192) + (u32)w * 1024 + lane * 16;
  const u32 o1 = sxh + (j == 0 ? 1 : 0) * 8192, o2 = sxh + (j == 2 ? 1 : 2) * 8192;
  v4u g1, g2;
  u32 op = 0;
  v4f A = {0.f, 0.f, 0.f, 0.f};
  {
    v4f Dq = {0.f, 0.f, 0.f, 0.f};
    VMWAIT(4);
    SB(); a[5] = LDNT(rsQ, voq, 5 * (QSTEP * HD * 2)); SB();
    xv0 |= xv0 << 16; xv1 |= xv1 << 16;
    Dq = sel_mfma(bcast8<0>(xv0), mk, a[0], Dq);
    asm volatile("" : "+s"(T));
    T += 1u;
    VMWAIT(4);
    SB(); a[6] = LDNT(rsQ, voq, 6 * (QSTEP * HD * 2)); SB();
    asm volatile("" : "+v"(Dq));
    Dq = sel_mfma(bcast8<1>(xv0), mk, a[1], Dq);
    VMWAIT(4);
    SB(); a[7] = LDNT(rsQ, voq, 7 * (QSTEP * HD * 2)); SB();
    asm volatile("" : "+v"(Dq));
    Dq = sel_mfma(bcast8<2>(xv0), mk, a[2], Dq);
    VMWAIT(4);
    SB(); a[8] = LDNT(rsQ, voq, 8 * (QSTEP * HD * 2)); SB();
    asm volatile("" : "+v"(Dq));
    Dq = sel_mfma(bcast8<3>(xv0), mk, a[3], Dq);
    VMWAIT(4);
    SB(); a[9] = LDNT(rsQ, voq, 9 * (QSTEP * HD * 2)); SB();
    asm volatile("" : "+v"(Dq));
    Dq = sel_mfma(bcast8<4>(xv0), mk, a[4], Dq);
    VMWAIT(4);
    SB(); kk[0] = LDNT(rsK, vok, 0); SB();
    asm volatile("" : "+v"(Dq));
    Dq = sel_mfma(bcast8<5>(xv0), mk, a[5], Dq);
    VMWAIT(4);
    SB(); kk[1] = LDNT(rsK, vok, 3072); SB();
    asm volatile("" : "+v"(Dq));
    Dq = sel_mfma(bcast8<6>(xv0), mk, a[6], Dq);
    VMWAIT(4);
    SB(); kk[2] = LDNT(rsK, vok, 6144); SB();
    asm volatile("" : "+v"(Dq));
    Dq = sel_mfma(bcast8<7>(xv0), mk, a[7], Dq);
    VMWAIT(4);
    SB(); kk[3] = LDNT(rsK, vok, 9216); SB();
    asm volatile("" : "+v"(Dq));
    Dq = sel_mfma(bcast8<0>(xv1), mk, a[8], Dq);
    VMWAIT(4);
    tnow = __builtin_amdgcn_s_memrealtime(); SB();
    asm volatile("" : "+v"(Dq));
    Dq = sel_mfma(bcast8<1>(xv1), mk, a[9], Dq);
    STAMP(1);
    if (((lane >> 3) & 1) == (lane >> 5)) *(LAS v4f*)&L.qp[2 * w + (lane >> 5)][8 * c8 + 4 * ((lane >> 4) & 1)] = Dq;
    if (lds_inc((LAS u32*)&L.cnt[0], lane) == 7u) {
      float q = 0.f;
#pragma unroll
      for (int i = 0; i < 16; ++i) q += *(volatile LAS float*)&L.qp[i][lane];
      ((LAS u16*)&L.qbf[0])[lane] = (u16)bfbits(q);
      asm volatile("" ::: "memory");
      if (lane == 0) *(volatile LAS u32*)&L.cnt[2] = 1u;
      asm volatile("" ::: "memory");
    }
    SB();
  }
  STAMP(2);
  VMWAIT(3);
  { const u32 el = (u32)tnow - tstart; lvl = __builtin_amdgcn_readfirstlane((el > 282u ? 1u : 0u) + (el > 322u ? 1u : 0u)); } SB();
  AWAIT(4, 3, 2);
  SB(); kk[4] = LDNT(rsK, vok, 12288); SB();
  AWAIT(4, 3, 2);
  SB(); kk[5] = LDNT(rsK, vok, 15360); SB();
  AWAIT(4, 3, 2);
  SB(); kk[6] = LDNT(rsK, vok, 18432); SB();
  tnow = __builtin_amdgcn_s_memrealtime(); SB();
  AWAIT(4, 3, 2);
  SB(); kk[7] = LDNT(rsK, vok, 21504); SB();
  { const u32 el = (u32)tnow - tstart; lvl = __builtin_amdgcn_readfirstlane((el > 360u ? 1u : 0u) + (el > 400u ? 1u : 0u)); } SB();
  STAMP(14);
  AWAIT(4, 3, 2);
  SB(); kk[8] = LDNT(rsK, vok, 24576); SB();
  AWAIT(4, 3, 2);
  SB(); kk[9] = LDNT(rsK, vok, 27648); SB();
  STAMP(15);
  AWAIT(4, 3, 2);
  SB(); kk[10] = LDNT(rsK, vok, 30720); SB();
  tnow = __builtin_amdgcn_s_memrealtime(); SB();
  AWAIT(4, 3, 2);
  SB(); kk[11] = LDNT(rsK, vok, 33792); SB();
  { const u32 el = (u32)tnow - tstart; lvl = __builtin_amdgcn_readfirstlane((el > 440u ? 1u : 0u) + (el > 480u ? 1u : 0u)); } SB();
  AWAIT(4, 3, 2);
  SB(); kk[12] = LDNT(rsK, vok, 36864); SB();
  AWAIT(4, 3, 2);
  SB(); kk[13] = LDNT(rsK, vok, 39936); SB();
  AWAIT(4, 3, 2);
  SB(); kk[14] = LDNT(rsK, vok, 43008); SB();
  tnow = __builtin_amdgcn_s_memrealtime(); SB();
  STAMP(13);
  AWAIT(4, 3, 2);
  SB(); kk[15] = LDNT(rsK, vok, 46080); SB();
  { const u32 el = (u32)tnow - tstart; lvl = __builtin_amdgcn_readfirstlane((el > 522u ? 1u : 0u) + (el > 562u ? 1u : 0u)); } SB();
  lds_wait((LAS u32*)&L.cnt[2], 1u);
  STAMP(3);
  float s0, s1;
  {
    const v4u qa = *(LAS v4u*)&L.qbf[4 * c8];
    float d[16];
    d[0] = dot8(kk[0], qa);
    d[1] = dot8(kk[1], qa);
    d[2] = dot8(kk[2], qa);
    d[3] = dot8(kk[3], qa);
    d[4] = dot8(kk[4], qa);
    d[5] = dot8(kk[5], qa);
    asm volatile("" : "+v"(d[5]));
    AWAIT(4, 3, 2);
    SB(); vv[0] = LDNT(rsV, vok, 0); SB();
    d[6] = dot8(kk[6], qa);
    d[7] = dot8(kk[7], qa);
    d[8] = dot8(kk[8], qa);
    d[9] = dot8(kk[9], qa);
    d[10] = dot8(kk[10], qa);
    d[11] = dot8(kk[11], qa);
    asm volatile("" : "+v"(d[11]));
    AWAIT(4, 3, 2);
    SB(); vv[1] = LDNT(rsV, vok, 3072); SB();
    d[12] = dot8(kk[12], qa);
    d[13] = dot8(kk[13], qa);
    d[14] = dot8(kk[14], qa);
    AWAIT(4, 3, 2);
    SB(); vv[2] = LDNT(rsV, vok, 6144); SB();
    tnow = __builtin_amdgcn_s_memrealtime(); SB();
    d[15] = dot8(kk[15], qa);
    float e[8], f[4];
#pragma unroll
    for (int i = 0; i < 8; ++i) { const float keep = b2 ? d[i + 8] : d[i], send = b2 ? d[i] : d[i + 8]; e[i] = keep + dpp<DPP_HMIR>(send); }
#pragma unroll
    for (int i = 0; i < 4; ++i) { const float keep = b0 ? e[i + 4] : e[i], send = b0 ? e[i] : e[i + 4]; f[i] = keep + dpp<DPP_XOR1>(send); }
    { const float keep = b1 ? f[2] : f[0], send = b1 ? f[0] : f[2]; s0 = keep + dpp<DPP_XOR2>(send); }
    { const float keep = b1 ? f[3] : f[1], send = b1 ? f[1] : f[3]; s1 = keep + dpp<DPP_XOR2>(send); }
    __builtin_amdgcn_raw_buffer_store_b128(v4u{__float_as_uint(s0), T, __float_as_uint(s1), T}, rsW, (int)(sxh + j * 8192), 0, AUX_SC1);
    SB();
  }
  STAMP(4);
  AWAIT(5, 4, 3);
  SB(); vv[3] = LDNT(rsV, vok, 9216); SB();
  { const u32 el = (u32)tnow - tstart; lvl = __builtin_amdgcn_readfirstlane((el > 610u ? 1u : 0u) + (el > 650u ? 1u : 0u)); } SB();
  AWAIT(5, 4, 3);
  SB(); vv[4] = LDNT(rsV, vok, 12288); SB();
  AWAIT(5, 4, 3);
  SB(); vv[5] = LDNT(rsV, vok, 15360); SB();
  AWAIT(5, 4, 2);
  SB(); vv[6] = LDNT(rsV, vok, 18432); SB();
  tnow = __builtin_amdgcn_s_memrealtime(); SB();
  AWAIT(5, 3, 2);
  SB(); vv[7] = LDNT(rsV, vok, 21504); SB();
  { const u32 el = (u32)tnow - tstart; lvl = __builtin_amdgcn_readfirstlane((el > 720u ? 1u : 0u) + (el > 760u ? 1u : 0u)); } SB();
  AWAIT(4, 3, 2);
  SB(); vv[8] = LDNT(rsV, vok, 24576); SB();
  AWAIT(4, 3, 2);
  SB(); vv[9] = LDNT(rsV, vok, 27648); SB();
  AWAIT(4, 3, 2);
  SB(); vv[10] = LDNT(rsV, vok, 30720); SB();
  tnow = __builtin_amdgcn_s_memrealtime(); SB();
  AWAIT(4, 3, 2);
  SB(); vv[11] = LDNT(rsV, vok, 33792); SB();
  { const u32 el = (u32)tnow - tstart; lvl = __builtin_amdgcn_readfirstlane((el > 815u ? 1u : 0u) + (el > 855u ? 1u : 0u)); } SB();
  AWAIT(4, 3, 2);
  SB(); vv[12] = LDNT(rsV, vok, 36864); SB();
  g1 = LDSC(rsW, o1); g2 = LDSC(rsW, o2); SB();
  AWAIT(6, 5, 4);
  SB(); vv[13] = LDNT(rsV, vok, 39936); SB();
  AWAIT(6, 5, 4);
  SB(); vv[14] = LDNT(rsV, vok, 43008); SB();
  tnow = __builtin_amdgcn_s_memrealtime(); SB();
  AWAIT(6, 5, 4);
  SB(); vv[15] = LDNT(rsV, vok, 46080); SB();
  { const u32 el = (u32)tnow - tstart; lvl = __builtin_amdgcn_readfirstlane((el > 910u ? 1u : 0u) + (el > 950u ? 1u : 0u)); } SB();
  STAMP(5);
  if (__builtin_expect(__builtin_amdgcn_ballot_w64(!((g1[1] == T) & (g1[3] == T) & (g2[1] == T) & (g2[3] == T))) != 0, 0)) {
    for (u32 spin = 0; spin < SPIN_CAP; ++spin) {
      asm volatile("" ::: "memory");
      u32 p1 = o1, p2 = o2;
      asm volatile("" : "+v"(p1), "+v"(p2));
      g1 = LDSC(rsW, p1); g2 = LDSC(rsW, p2);
      const bool ok = (g1[1] == T) & (g1[3] == T) & (g2[1] == T) & (g2[3] == T);
      if (__builtin_amdgcn_ballot_w64(!ok) == 0) break;
    }
  }
  STAMP(6);
  SB();
  float m, ls;
  v4u pv[4];
  {
    const float x0 = j == 0 ? s0 : __uint_as_float(g1[0]), x1 = j == 1 ? s0 : __uint_as_float(j == 0 ? g1[0] : g2[0]), x2 = j == 2 ? s0 : __uint_as_float(g2[0]);
    const float y0 = j == 0 ? s1 : __uint_as_float(g1[2]), y1 = j == 1 ? s1 : __uint_as_float(j == 0 ? g1[2] : g2[2]), y2 = j == 2 ? s1 : __uint_as_float(g2[2]);
    const float sa = rbf(rbf((x0 + x1) + x2) * SCALE), sb = rbf(rbf((y0 + y1) + y2) * SCALE);
    m = fmaxf(sa, sb);
    m = fmaxf(m, dpp<DPP_XOR1>(m));
    m = fmaxf(m, dpp<DPP_XOR2>(m));
    m = fmaxf(m, dpp<DPP_HMIR>(m));
    m = fmaxf(m, dpp<DPP_ROR8>(m));
    m = max_rows(m);
    const float pa = __builtin_amdgcn_exp2f((sa - m) * LOG2E), pb = __builtin_amdgcn_exp2f((sb - m) * LOG2E);
    *(LAS v2u*)&L.pbuf[w][kr * 16 + (b2 ? 8 : 0) + (b0 ? 4 : 0) + (b1 ? 2 : 0)] = v2u{dup_bf(pa), dup_bf(pb)};
    asm volatile("" ::: "memory");
#pragma unroll
    for (int k = 0; k < 4; ++k) pv[k] = *(LAS v4u*)&L.pbuf[w][kr * 16 + 4 * k];
    ls = pa + pb;
    ls += dpp<DPP_XOR1>(ls);
    ls += dpp<DPP_XOR2>(ls);
    ls += dpp<DPP_HMIR>(ls);
    ls += dpp<DPP_ROR8>(ls);
    ls = sum_rows(ls);
  }
  {
    v4f Dv = {0.f, 0.f, 0.f, 0.f};
    Dv = sel_mfma(pv[0][0], mk, vv[0], Dv);
    Dv = sel_mfma(pv[0][1], mk, vv[1], Dv);
    Dv = sel_mfma(pv[0][2], mk, vv[2], Dv);
    Dv = sel_mfma(pv[0][3], mk, vv[3], Dv);
    Dv = sel_mfma(pv[1][0], mk, vv[4], Dv);
    Dv = sel_mfma(pv[1][1], mk, vv[5], Dv);
    Dv = sel_mfma(pv[1][2], mk, vv[6], Dv);
    Dv = sel_mfma(pv[1][3], mk, vv[7], Dv);
    Dv = sel_mfma(pv[2][0], mk, vv[8], Dv);
    Dv = sel_mfma(pv[2][1], mk, vv[9], Dv);
    Dv = sel_mfma(pv[2][2], mk, vv[10], Dv);
    Dv = sel_mfma(pv[2][3], mk, vv[11], Dv);
    Dv = sel_mfma(pv[3][0], mk, vv[12], Dv);
    asm volatile("" : "+v"(Dv));
    AWAIT(6, 5, 2);
    SB(); ww[0] = LDNT(rsO, voo, 0); SB();
    Dv = sel_mfma(pv[3][1], mk, vv[13], Dv);
    asm volatile("" : "+v"(Dv));
    AWAIT(6, 3, 2);
    SB(); ww[1] = LDNT(rsO, voo, 5120); SB();
    Dv = sel_mfma(pv[3][2], mk, vv[14], Dv);
    asm volatile("" : "+v"(Dv));
    AWAIT(4, 3, 2);
    SB(); ww[2] = LDNT(rsO, voo, 10240); SB();
    SB(); ww[3] = LDNT(rsO, voo, 15360); SB();
    SB(); ww[4] = LDNT(rsO, voo, 20480); SB();
    SB(); ww[5] = LDNT(rsO, voo, 25600); SB();
    SB(); ww[6] = LDNT(rsO, voo, 30720); SB();
    SB(); ww[7] = LDNT(rsO, voo, 35840); SB();
    Dv = sel_mfma(pv[3][3], mk, vv[15], Dv);
    if (((lane >> 3) & 1) == (lane >> 5)) *(LAS v4f*)&L.opart[w][lane >> 5][8 * c8 + 4 * ((lane >> 4) & 1)] = Dv;
    if (lane == 0) *(LAS v2f*)&L.ml[w][0] = v2f{m, ls};
    lds_inc((LAS u32*)&L.cnt[3], lane);
  }
  STAMP(7);
  if (w >= 3) {
    VMWAIT(7);
    SB(); ww[8] = LDNT(rsO, voo, 40960); SB();
    VMWAIT(7);
    SB(); ww[9] = LDNT(rsO, voo, 46080); SB();
    VMWAIT(7);
    SB(); ww[10] = LDNT(rsO, voo, 51200); SB();
    VMWAIT(7);
    SB(); ww[11] = LDNT(rsO, voo, 56320); SB();
    VMWAIT(7);
    SB(); ww[12] = LDNT(rsO, voo, 61440); SB();
    VMWAIT(7);
    SB(); ww[13] = LDNT(rsO, voo, 66560); SB();
    VMWAIT(7);
    SB(); ww[14] = LDNT(rsO, voo, 71680); SB();
    VMWAIT(7);
    SB(); ww[15] = LDNT(rsO, voo, 76800); SB();
    lds_wait((LAS u32*)&L.cnt[3], 8u);
    const int col = 4 * (lane & 15) + (lane >> 4);
    float ov[8];
#pragma unroll
    for (int i = 0; i < 8; ++i) ov[i] = L.opart[i][0][col] + L.opart[i][1][col];
    const v2f ml = *(LAS v2f*)&L.ml[c8][0];
    float M = ml[0];
    M = fmaxf(M, dpp<DPP_XOR1>(M));
    M = fmaxf(M, dpp<DPP_XOR2>(M));
    M = fmaxf(M, dpp<DPP_HMIR>(M));
    const float f = __builtin_amdgcn_exp2f((ml[0] - M) * LOG2E);
    float ln = f * ml[1];
    ln += dpp<DPP_XOR1>(ln);
    ln += dpp<DPP_XOR2>(ln);
    ln += dpp<DPP_HMIR>(ln);
    float on = dpp<DPP_BCAST + 0>(f) * ov[0];
    on = __builtin_fmaf(dpp<DPP_BCAST + 1>(f), ov[1], on);
    on = __builtin_fmaf(dpp<DPP_BCAST + 2>(f), ov[2], on);
    on = __builtin_fmaf(dpp<DPP_BCAST + 3>(f), ov[3], on);
    on = __builtin_fmaf(dpp<DPP_BCAST + 4>(f), ov[4], on);
    on = __builtin_fmaf(dpp<DPP_BCAST + 5>(f), ov[5], on);
    on = __builtin_fmaf(dpp<DPP_BCAST + 6>(f), ov[6], on);
    on = __builtin_fmaf(dpp<DPP_BCAST + 7>(f), ov[7], on);
    op = dup_bf(on * __builtin_amdgcn_rcpf(ln));
    STAMP(8);
    A = sel_mfma(dppu<DPP_BCAST + 0>(op), mk, ww[0], A);
    A = sel_mfma(dppu<DPP_BCAST + 1>(op), mk, ww[1], A);
    A = sel_mfma(dppu<DPP_BCAST + 2>(op), mk, ww[2], A);
    A = sel_mfma(dppu<DPP_BCAST + 3>(op), mk, ww[3], A);
    A = sel_mfma(dppu<DPP_BCAST + 4>(op), mk, ww[4], A);
    A = sel_mfma(dppu<DPP_BCAST + 5>(op), mk, ww[5], A);
    A = sel_mfma(dppu<DPP_BCAST + 6>(op), mk, ww[6], A);
    A = sel_mfma(dppu<DPP_BCAST + 7>(op), mk, ww[7], A);
    asm volatile("" : "+v"(A));
    VMWAIT(7);
    A = sel_mfma(dppu<DPP_BCAST + 8>(op), mk, ww[8], A);
    asm volatile("" : "+v"(A));
    VMWAIT(6);
    A = sel_mfma(dppu<DPP_BCAST + 9>(op), mk, ww[9], A);
    asm volatile("" : "+v"(A));
    VMWAIT(5);
    A = sel_mfma(dppu<DPP_BCAST + 10>(op), mk, ww[10], A);
    asm volatile("" : "+v"(A));
    VMWAIT(4);
    A = sel_mfma(dppu<DPP_BCAST + 11>(op), mk, ww[11], A);
    asm volatile("" : "+v"(A));
    VMWAIT(3);
    A = sel_mfma(dppu<DPP_BCAST + 12>(op), mk, ww[12], A);
    asm volatile("" : "+v"(A));
    VMWAIT(2);
    A = sel_mfma(dppu<DPP_BCAST + 13>(op), mk, ww[13], A);
    asm volatile("" : "+v"(A));
    VMWAIT(1);
    A = sel_mfma(dppu<DPP_BCAST + 14>(op), mk, ww[14], A);
    asm volatile("" : "+v"(A));
    VMWAIT(0);
    A = sel_mfma(dppu<DPP_BCAST + 15>(op), mk, ww[15], A);
    STAMP(10);
  }
  if (w >= 3) {
    {
      const bool up = lane >= 32;
      const float v0 = up ? A[2] : A[0], v1 = up ? A[3] : A[1];
      const u32 off = FX_OFF + (u32)((p * 80 + 16 * (w - 3) + (lane & 15)) * 96 + li) * 64 + (2 * ((lane >> 4) & 1) + (lane >> 5)) * 16;
      __builtin_amdgcn_raw_buffer_store_b128(v4u{__float_as_uint(v0), T, __float_as_uint(v1), T}, rsW, (int)off, 0, AUX_SC1);
    }
    if (li < 80) lds_inc((LAS u32*)&L.cnt[4], lane);
    STAMP(11);
  } else if (li < 80) {
    lds_wait((LAS u32*)&L.cnt[4], 5u);
    const u32 base = FX_OFF + (u32)(p * 80 + li) * 6144 + lane * 16;
    v4u g[6];
    if (w == 1) __builtin_amdgcn_s_sleep(PSL);
    if (w == 2) __builtin_amdgcn_s_sleep(2 * PSL);
#pragma unroll
    for (int k = 0; k < 6; ++k) g[k] = LDSC(rsW, base + k * 1024);
    for (u32 spin = 0; spin < SPIN_CAP; ++spin) {
      u32 pb = base;
      asm volatile("" : "+v"(pb) :: "memory");
      bool all = true;
#pragma unroll
      for (int k = 0; k < 6; ++k) all = all & (g[k][1] == T) & (g[k][3] == T);
      if (__builtin_amdgcn_ballot_w64(!all) == 0) break;
#pragma unroll
      for (int k = 0; k < 6; ++k) if (!((g[k][1] == T) & (g[k][3] == T))) g[k] = LDSC(rsW, pb + k * 1024);
    }
    float v0 = 0.f, v1 = 0.f;
#pragma unroll
    for (int k = 0; k < 6; ++k) { v0 += __uint_as_float(g[k][0]); v1 += __uint_as_float(g[k][2]); }
    v0 += dpp<DPP_ROR8>(v0); v1 += dpp<DPP_ROR8>(v1);
    v0 += dpp<DPP_ROR4>(v0); v1 += dpp<DPP_ROR4>(v1);
    v0 = sum_rows(v0); v1 = sum_rows(v1);
    if (lane < 4) ((u32*)out)[(p * D + li * 8) / 2 + lane] = bfbits(v0) | (bfbits(v1) << 16);
    if (li == 0 && lane == 0) __builtin_amdgcn_raw_buffer_store_b32(T, rsW, p * 128, 0, AUX_SC1);
    STAMP(12);
  }
  STAMP_FLUSH();
}
}

extern "C" void kernel_launch(void* const* d_in, const int* in_sizes, int n_in, void* d_out, int out_size, void* d_ws, size_t ws_size,
                              hipStream_t stream) {
  fused_decode<<<192, NTHR, 0, stream>>>((const u16*)d_in[0], (const u16*)d_in[1], (const u16*)d_in[2], (const u16*)d_in[3], (const u16*)d_in[4],
                                         (const u16*)d_in[5], (const u16*)d_in[6], (const u16*)d_in[7], (const u16*)d_in[8], (const u16*)d_in[9],
                                         (u16*)d_out, (unsigned char*)d_ws);
}
```
